```python
import torch

def _softmax(x, axis=-1):
    return torch.softmax(x, dim=axis)
def _gelu(x):
    return torch.nn.functional.gelu(x, approximate="tanh")
def _silu(x):
    return torch.nn.functional.silu(x)
def _sigmoid(x):
    return torch.sigmoid(x)
def _layernorm(x, axis=-1, eps=1e-5):
    m = x.mean(dim=axis, keepdim=True)
    v = x.var(dim=axis, keepdim=True, unbiased=False)
    return (x - m) / torch.sqrt(v + eps)
def _scatter_add(tgt, idx, src, axis=0):
    return tgt.clone().index_add_(axis, idx, src)
def _segment_sum(src, seg, n):
    out = torch.zeros((n,) + src.shape[1:], dtype=src.dtype)
    return out.index_add_(0, seg, src)
def _pad(x, pw):
    flat = tuple(v for pair in reversed(pw) for v in pair)
    return torch.nn.functional.pad(x, flat)

kernel_name = 'recurrent_scan__spectral_fnet__glu_gated_glu__prompt'

def setup_inputs():

    _w = (lambda a: (lambda t: t.to(torch.bfloat16) if t.is_floating_point() else t)(torch.as_tensor(a)))
    def _A_setup():
        B, S, D = 1, 2048, 576
        ns = torch.arange(S)[:, None] * torch.arange(S)[None, :]
        nd = torch.arange(D)[:, None] * torch.arange(D)[None, :]
        Ws_re = torch.cos(-2.0 * torch.pi * ns / S).to(float)
        Ws_im = torch.sin(-2.0 * torch.pi * ns / S).to(float)
        Wd_re = torch.cos(-2.0 * torch.pi * nd / D).to(float)
        Wd_im = torch.sin(-2.0 * torch.pi * nd / D).to(float)
        return {
            "x": _w((lambda *s: torch.randn(*s, dtype=torch.bfloat16) * 0.05)(B, S, D)),
            "dft_seq_re": _w(Ws_re),
            "dft_seq_im": _w(Ws_im),
            "dft_hid_re": _w(Wd_re),
            "dft_hid_im": _w(Wd_im),
        }
    _A_in = _A_setup()

    def _B_setup():
        B, S, D, F = 1, 2048, 576, 4608
        return {
            "x": _w((lambda *s: torch.randn(*s, dtype=torch.bfloat16) * 0.05)(B, S, D)),
            "w_gate": _w((lambda *s: torch.randn(*s, dtype=torch.bfloat16) * 0.05)(D, F)),
            "w_up": _w((lambda *s: torch.randn(*s, dtype=torch.bfloat16) * 0.05)(D, F)),
            "w_down": _w((lambda *s: torch.randn(*s, dtype=torch.bfloat16) * 0.05)(F, D)),
        }
    _B_in = _B_setup()

    return {
        "A_x": _A_in["x"],
        "A_dft_seq_re": _A_in["dft_seq_re"],
        "A_dft_seq_im": _A_in["dft_seq_im"],
        "A_dft_hid_re": _A_in["dft_hid_re"],
        "A_dft_hid_im": _A_in["dft_hid_im"],
        "B_x": _B_in["x"],
        "B_w_gate": _B_in["w_gate"],
        "B_w_up": _B_in["w_up"],
        "B_w_down": _B_in["w_down"],
    }

def reference(A_x, A_dft_seq_re, A_dft_seq_im, A_dft_hid_re, A_dft_hid_im, B_x, B_w_gate, B_w_up, B_w_down):
    def _A_ref(x, dft_seq_re, dft_seq_im, dft_hid_re, dft_hid_im):
        a = torch.einsum("bsd,ed->bse", x, dft_hid_re)
        b = torch.einsum("bsd,ed->bse", x, dft_hid_im)
        y = torch.einsum("ts,bse->bte", dft_seq_re, a) - torch.einsum("ts,bse->bte", dft_seq_im, b)
        return y

    def _B_ref(x, w_gate, w_up, w_down):
        g = torch.einsum("bsd,df->bsf", x, w_gate)
        u = torch.einsum("bsd,df->bsf", x, w_up)
        h = _sigmoid(g) * u
        return torch.einsum("bsf,fd->bsd", h, w_down)

    x = A_x
    xn = _layernorm(x, axis=-1)
    s = _A_ref(xn, A_dft_seq_re, A_dft_seq_im, A_dft_hid_re, A_dft_hid_im)
    g = _silu(_B_ref(xn, B_w_gate, B_w_up, B_w_down))
    return x + s * g
```

```cpp
#include <hip/hip_runtime.h>
#include <hip/hip_bf16.h>

typedef unsigned short u16;
typedef __attribute__((ext_vector_type(8))) short bf16x8;
typedef __attribute__((ext_vector_type(4))) short bf16x4;
typedef __attribute__((ext_vector_type(4))) float f32x4;
typedef __attribute__((ext_vector_type(4))) unsigned short u16x4;
typedef __attribute__((ext_vector_type(8))) unsigned short u16x8;
typedef __attribute__((address_space(3))) bf16x4 lds_bf16x4;

constexpr int S_ = 2048, D_ = 576, F_ = 4608;

__device__ __forceinline__ float bf2f(u16 v) { return __uint_as_float(((unsigned)v) << 16); }
__device__ __forceinline__ u16 f2bf(float f) { __bf16 b = (__bf16)f; return __builtin_bit_cast(u16, b); }
__device__ __forceinline__ float rbf(float f) { return bf2f(f2bf(f)); }
__device__ __forceinline__ float sigmoidf_(float x) { return 1.0f / (1.0f + __expf(-x)); }

__device__ __forceinline__ void glds16(const void* g, void* l) {
  __builtin_amdgcn_global_load_lds((const unsigned*)g, (unsigned*)l, 16, 0, 0);
}
__device__ __forceinline__ bf16x4 tr_read(const char* p) {
  return __builtin_amdgcn_ds_read_tr16_b64_v4i16((lds_bf16x4*)p);
}
__device__ __forceinline__ bf16x8 cat2(bf16x4 lo, bf16x4 hi) {
  return __builtin_shufflevector(lo, hi, 0, 1, 2, 3, 4, 5, 6, 7);
}
#define MFMA16(a, b, c) __builtin_amdgcn_mfma_f32_16x16x32_bf16((a), (b), (c), 0, 0, 0)
#define WAIT_V0() asm volatile("s_waitcnt vmcnt(0)" ::: "memory")

constexpr int K0_ET = 64;
constexpr int K0_LDS = K0_ET * D_ * 2;
__global__ __launch_bounds__(512) void k0_ln_ab(const u16* __restrict__ x, const u16* __restrict__ wre,
                                                const u16* __restrict__ wim, u16* __restrict__ xn,
                                                u16* __restrict__ a, u16* __restrict__ nb) {
  extern __shared__ __attribute__((aligned(16))) char smem[];
  const int tid = threadIdx.x, lane = tid & 63, wave = tid >> 6;
  const int et = blockIdx.x % 18, st = blockIdx.x / 18;
  const bool isim = et >= 9;
  const u16* W = (isim ? wim : wre) + (size_t)((et % 9) * K0_ET) * D_;
#pragma unroll
  for (int j = 0; j < 9; ++j) {
    int piece = wave * 9 + j;
    int p = piece * 64 + lane;
    int row = p / 72, cp = p % 72;
    int c = cp ^ (row & 7);
    glds16(W + (size_t)row * D_ + c * 8, smem + piece * 1024);
  }
  const int fr = lane & 15, fq = lane >> 4;
  const int row = st * 128 + wave * 16 + fr;
  const u16* xr = x + (size_t)row * D_ + fq * 8;
  u16x8 xv[18];
#pragma unroll
  for (int s = 0; s < 18; ++s) xv[s] = *(const u16x8*)(xr + s * 32);
  float sum = 0.f;
#pragma unroll
  for (int s = 0; s < 18; ++s)
#pragma unroll
    for (int j = 0; j < 8; ++j) sum += bf2f(xv[s][j]);
  sum += __shfl_xor(sum, 16);
  sum += __shfl_xor(sum, 32);
  const float mean = sum * (1.0f / D_);
  float vs = 0.f;
#pragma unroll
  for (int s = 0; s < 18; ++s)
#pragma unroll
    for (int j = 0; j < 8; ++j) { float d = bf2f(xv[s][j]) - mean; vs += d * d; }
  vs += __shfl_xor(vs, 16);
  vs += __shfl_xor(vs, 32);
  const float mb = rbf(mean);
  const float vb = rbf(vs * (1.0f / D_));
  const float den = rbf(sqrtf(rbf(vb + 1e-5f)));
  bf16x8 xf[18];
#pragma unroll
  for (int s = 0; s < 18; ++s) {
    u16x8 t;
#pragma unroll
    for (int j = 0; j < 8; ++j) t[j] = f2bf(rbf(bf2f(xv[s][j]) - mb) / den);
    xf[s] = __builtin_bit_cast(bf16x8, t);
  }
  if (et == 0) {
    u16* xo = xn + (size_t)row * D_ + fq * 8;
#pragma unroll
    for (int s = 0; s < 18; ++s) *(bf16x8*)(xo + s * 32) = xf[s];
  }
  WAIT_V0();
  __syncthreads();
  f32x4 acc[4] = {};
#pragma unroll
  for (int s = 0; s < 18; ++s) {
#pragma unroll
    for (int t = 0; t < 4; ++t) {
      int r = t * 16 + fr;
      int c = s * 4 + fq;
      bf16x8 wf = *(const bf16x8*)(smem + r * 1152 + ((c ^ (r & 7)) * 16));
      acc[t] = MFMA16(wf, xf[s], acc[t]);
    }
  }
  u16* o = (isim ? nb : a) + (size_t)row * D_ + (et % 9) * K0_ET + fq * 4;
  const float sg = isim ? -1.f : 1.f;
#pragma unroll
  for (int t = 0; t < 4; ++t) {
    u16x4 v;
#pragma unroll
    for (int r = 0; r < 4; ++r) v[r] = f2bf(sg * acc[t][r]);
    *(u16x4*)(o + t * 16) = v;
  }
}

constexpr int K1_XB = 256 * 64 * 2;
constexpr int K1_WB = 64 * 288 * 2;
constexpr int K1_STG = K1_XB + K1_WB;
constexpr int K1_LDS = 2 * K1_STG;
__global__ __launch_bounds__(768) void k1_glu(const u16* __restrict__ xn, const u16* __restrict__ wg,
                                              const u16* __restrict__ wu, u16* __restrict__ h) {
  extern __shared__ __attribute__((aligned(16))) char smem[];
  const int tid = threadIdx.x, lane = tid & 63, wave = tid >> 6;
  const int ws = wave & 3, wf = wave >> 2;
  const int xcd = blockIdx.x & 7, idx = blockIdx.x >> 3;
  const int ft = xcd * 4 + (idx >> 3), st = idx & 7;
  const int f0 = ft * 144, s0 = st * 256;
  const int fr = lane & 15, fq = lane >> 4;
  const u16* src[6];
  long kstep[6];
#pragma unroll
  for (int j = 0; j < 6; ++j) {
    int i = wave + 12 * j;
    if (i < 32) {
      int r = i * 8 + (lane >> 3);
      int c = (lane & 7) ^ (lane >> 3);
      src[j] = xn + (size_t)(s0 + r) * D_ + c * 8;
      kstep[j] = 64;
    } else {
      int jj = (i < 68) ? (i - 32) : 0;
      int sb = jj * 4 + (lane >> 4), ii = lane & 15;
      int kb = sb / 18, nbk = sb % 18;
      int r = (ii >> 1) ^ ((kb & 1) << 2);
      const u16* Wm = (nbk < 9) ? wg : wu;
      src[j] = Wm + (size_t)(kb * 8 + r) * F_ + f0 + (nbk % 9) * 16 + (ii & 1) * 8;
      kstep[j] = 64L * F_;
    }
  }
  auto stage = [&](int buf, int kt) {
#pragma unroll
    for (int j = 0; j < 6; ++j) {
      int i = wave + 12 * j;
      if (j < 5 || wave < 8) glds16(src[j] + kt * kstep[j], smem + buf * K1_STG + i * 1024);
    }
  };
  f32x4 accg[3][4] = {}, accu[3][4] = {};
  const int q = (lane & 15) >> 2, p = lane & 3;
  const int sw = (fq & 1) << 2;
  stage(0, 0);
  for (int kt = 0; kt < 9; ++kt) {
    WAIT_V0();
    __syncthreads();
    if (kt + 1 < 9) stage((kt + 1) & 1, kt + 1);
    const char* xb = smem + (kt & 1) * K1_STG;
    const char* wb = xb + K1_XB;
#pragma unroll
    for (int ks = 0; ks < 2; ++ks) {
      bf16x8 xf[4], gf[3], uf[3];
#pragma unroll
      for (int m = 0; m < 4; ++m) {
        int r = ws * 64 + m * 16 + fr;
        int c = ks * 4 + fq;
        xf[m] = *(const bf16x8*)(xb + r * 128 + ((c ^ (r & 7)) * 16));
      }
#pragma unroll
      for (int t = 0; t < 3; ++t) {
        int kb = ks * 4 + fq;
        const char* bg = wb + (kb * 18 + wf * 3 + t) * 256 + p * 8;
        const char* bu = bg + 9 * 256;
        gf[t] = cat2(tr_read(bg + ((q ^ sw) * 32)), tr_read(bg + (((q + 4) ^ sw) * 32)));
        uf[t] = cat2(tr_read(bu + ((q ^ sw) * 32)), tr_read(bu + (((q + 4) ^ sw) * 32)));
      }
#pragma unroll
      for (int t = 0; t < 3; ++t)
#pragma unroll
        for (int m = 0; m < 4; ++m) {
          accg[t][m] = MFMA16(gf[t], xf[m], accg[t][m]);
          accu[t][m] = MFMA16(uf[t], xf[m], accu[t][m]);
        }
    }
  }
#pragma unroll
  for (int t = 0; t < 3; ++t)
#pragma unroll
    for (int m = 0; m < 4; ++m) {
      u16x4 v;
#pragma unroll
      for (int r = 0; r < 4; ++r) {
        float g = rbf(accg[t][m][r]), u = rbf(accu[t][m][r]);
        v[r] = f2bf(rbf(sigmoidf_(g)) * u);
      }
      int srow = s0 + ws * 64 + m * 16 + fr;
      *(u16x4*)(h + (size_t)srow * F_ + f0 + wf * 48 + t * 16 + fq * 4) = v;
    }
}

constexpr int K2_AB = 128 * 128 * 2;
constexpr int K2_BB = 128 * 144 * 2;
constexpr int K2_STG = K2_AB + K2_BB;
constexpr int K2_LDS = 4 * 36864;
__global__ __launch_bounds__(512) void k2_gemm(const u16* __restrict__ sre, const u16* __restrict__ sim,
                                               const u16* __restrict__ a, const u16* __restrict__ nb,
                                               const u16* __restrict__ h, const u16* __restrict__ wd,
                                               u16* __restrict__ yre, u16* __restrict__ ynim,
                                               float* __restrict__ olo, float* __restrict__ ohi) {
  extern __shared__ __attribute__((aligned(16))) char smem[];
  const int tid = threadIdx.x, lane = tid & 63, wave = tid >> 6;
  const int wt = wave & 1, wk = wave >> 1;
  const int xcd = blockIdx.x & 7, idx = blockIdx.x >> 3;
  const int sp = xcd >> 1;
  const int mt = (xcd & 1) * 8 + (idx >> 2), nt = idx & 3;
  const int t0 = mt * 128, e0 = nt * 144;
  const int fr = lane & 15, fq = lane >> 4;
  const u16* A; const u16* B; long lda; int nst;
  if (sp == 0) { A = sre; B = a; lda = S_; nst = 16; }
  else if (sp == 1) { A = sim; B = nb; lda = S_; nst = 16; }
  else if (sp == 2) { A = h; B = wd; lda = F_; nst = 18; }
  else { A = h + 2304; B = wd + (size_t)2304 * D_; lda = F_; nst = 18; }
  const u16* src[9];
  long kstep[9];
#pragma unroll
  for (int j = 0; j < 9; ++j) {
    int i = wave + 8 * j;
    if (i < 32) {
      int r = i * 4 + (lane >> 4);
      int c = (lane & 15) ^ (r & 15);
      src[j] = A + (size_t)(t0 + r) * lda + c * 8;
      kstep[j] = 128;
    } else {
      int jj = (i < 68) ? (i - 32) : 0;
      int sb = jj * 4 + (lane >> 4), ii = lane & 15;
      int kb = sb / 9, nbk = sb % 9;
      int r = (ii >> 1) ^ ((kb & 1) << 2);
      src[j] = B + (size_t)(kb * 8 + r) * D_ + e0 + nbk * 16 + (ii & 1) * 8;
      kstep[j] = 128L * D_;
    }
  }
  auto stage = [&](int buf, int kt) {
#pragma unroll
    for (int j = 0; j < 9; ++j) {
      int i = wave + 8 * j;
      if (j < 8 || wave < 4) glds16(src[j] + kt * kstep[j], smem + buf * K2_STG + i * 1024);
    }
  };
  f32x4 acc[9][4] = {};
  const int q = (lane & 15) >> 2, p = lane & 3;
  const int sw = (fq & 1) << 2;
  stage(0, 0);
  for (int kt = 0; kt < nst; ++kt) {
    WAIT_V0();
    __syncthreads();
    if (kt + 1 < nst) stage((kt + 1) & 1, kt + 1);
    const char* ab = smem + (kt & 1) * K2_STG;
    const char* bb = ab + K2_AB;
    bf16x8 af[4], bfr[9];
#pragma unroll
    for (int m = 0; m < 4; ++m) {
      int r = wt * 64 + m * 16 + fr;
      int c = wk * 4 + fq;
      af[m] = *(const bf16x8*)(ab + r * 256 + ((c ^ (r & 15)) * 16));
    }
#pragma unroll
    for (int n = 0; n < 9; ++n) {
      int kb = wk * 4 + fq;
      const char* bp = bb + (kb * 9 + n) * 256 + p * 8;
      bfr[n] = cat2(tr_read(bp + ((q ^ sw) * 32)), tr_read(bp + (((q + 4) ^ sw) * 32)));
    }
#pragma unroll
    for (int n = 0; n < 9; ++n)
#pragma unroll
      for (int m = 0; m < 4; ++m) acc[n][m] = MFMA16(bfr[n], af[m], acc[n][m]);
  }
  __syncthreads();
  if (wk >= 2) {
    char* dst = smem + ((wk - 2) * 2 + wt) * 36864 + lane * 16;
#pragma unroll
    for (int n = 0; n < 9; ++n)
#pragma unroll
      for (int m = 0; m < 4; ++m) *(f32x4*)(dst + (n * 4 + m) * 1024) = acc[n][m];
  }
  __syncthreads();
  if (wk < 2) {
    const char* s = smem + (wk * 2 + wt) * 36864 + lane * 16;
#pragma unroll
    for (int n = 0; n < 9; ++n)
#pragma unroll
      for (int m = 0; m < 4; ++m) acc[n][m] += *(const f32x4*)(s + (n * 4 + m) * 1024);
  }
  __syncthreads();
  if (wk == 1) {
    char* dst = smem + wt * 36864 + lane * 16;
#pragma unroll
    for (int n = 0; n < 9; ++n)
#pragma unroll
      for (int m = 0; m < 4; ++m) *(f32x4*)(dst + (n * 4 + m) * 1024) = acc[n][m];
  }
  __syncthreads();
  if (wk == 0) {
    const char* s = smem + wt * 36864 + lane * 16;
#pragma unroll
    for (int n = 0; n < 9; ++n)
#pragma unroll
      for (int m = 0; m < 4; ++m) acc[n][m] += *(const f32x4*)(s + (n * 4 + m) * 1024);
    if (sp < 2) {
      u16* o = (sp == 0 ? yre : ynim);
#pragma unroll
      for (int n = 0; n < 9; ++n)
#pragma unroll
        for (int m = 0; m < 4; ++m) {
          u16x4 v;
#pragma unroll
          for (int r = 0; r < 4; ++r) v[r] = f2bf(acc[n][m][r]);
          *(u16x4*)(o + (size_t)(t0 + wt * 64 + m * 16 + fr) * D_ + e0 + n * 16 + fq * 4) = v;
        }
    } else {
      float* o = (sp == 2 ? olo : ohi);
#pragma unroll
      for (int n = 0; n < 9; ++n)
#pragma unroll
        for (int m = 0; m < 4; ++m)
          *(f32x4*)(o + (size_t)(t0 + wt * 64 + m * 16 + fr) * D_ + e0 + n * 16 + fq * 4) = acc[n][m];
    }
  }
}

__global__ __launch_bounds__(256) void k3_combine(const u16* __restrict__ x, const u16* __restrict__ yre,
                                                  const u16* __restrict__ ynim, const float* __restrict__ olo,
                                                  const float* __restrict__ ohi, u16* __restrict__ out) {
  const size_t i = ((size_t)blockIdx.x * 256 + threadIdx.x) * 8;
  u16x8 xv = *(const u16x8*)(x + i), a = *(const u16x8*)(yre + i), b = *(const u16x8*)(ynim + i);
  f32x4 l0 = *(const f32x4*)(olo + i), l1 = *(const f32x4*)(olo + i + 4);
  f32x4 h0 = *(const f32x4*)(ohi + i), h1 = *(const f32x4*)(ohi + i + 4);
  u16x8 r;
#pragma unroll
  for (int j = 0; j < 8; ++j) {
    float s = rbf(bf2f(a[j]) + bf2f(b[j]));
    float o = rbf((j < 4 ? l0[j] : l1[j - 4]) + (j < 4 ? h0[j] : h1[j - 4]));
    float g = rbf(o * sigmoidf_(o));
    r[j] = f2bf(bf2f(xv[j]) + rbf(s * g));
  }
  *(u16x8*)(out + i) = r;
}

extern "C" void kernel_launch(void* const* d_in, const int* in_sizes, int n_in,
                              void* d_out, int out_size, void* d_ws, size_t ws_size,
                              hipStream_t stream) {
  const u16* x = (const u16*)d_in[0];
  const u16* sre = (const u16*)d_in[1];
  const u16* sim = (const u16*)d_in[2];
  const u16* wre = (const u16*)d_in[3];
  const u16* wim = (const u16*)d_in[4];
  const u16* wg = (const u16*)d_in[6];
  const u16* wu = (const u16*)d_in[7];
  const u16* wd = (const u16*)d_in[8];
  char* ws = (char*)d_ws;
  const size_t SD2 = (size_t)S_ * D_ * 2;
  u16* xn = (u16*)(ws);
  u16* a = (u16*)(ws + SD2);
  u16* nb = (u16*)(ws + 2 * SD2);
  u16* yre = (u16*)(ws + 3 * SD2);
  u16* ynim = (u16*)(ws + 4 * SD2);
  float* olo = (float*)(ws + 5 * SD2);
  float* ohi = (float*)(ws + 7 * SD2);
  u16* h = (u16*)(ws + 9 * SD2);
  hipFuncSetAttribute((const void*)k0_ln_ab, hipFuncAttributeMaxDynamicSharedMemorySize, K0_LDS);
  hipFuncSetAttribute((const void*)k1_glu, hipFuncAttributeMaxDynamicSharedMemorySize, K1_LDS);
  hipFuncSetAttribute((const void*)k2_gemm, hipFuncAttributeMaxDynamicSharedMemorySize, K2_LDS);
  k0_ln_ab<<<16 * 18, 512, K0_LDS, stream>>>(x, wre, wim, xn, a, nb);
  k1_glu<<<256, 768, K1_LDS, stream>>>(xn, wg, wu, h);
  k2_gemm<<<256, 512, K2_LDS, stream>>>(sre, sim, a, nb, h, wd, yre, ynim, olo, ohi);
  k3_combine<<<(S_ * D_ / 8) / 256, 256, 0, stream>>>(x, yre, ynim, olo, ohi, (u16*)d_out);
}
```

```cpp
#include <hip/hip_runtime.h>
#include <hip/hip_bf16.h>

typedef unsigned short u16;
typedef __attribute__((ext_vector_type(8))) short bf16x8;
typedef __attribute__((ext_vector_type(4))) short bf16x4;
typedef __attribute__((ext_vector_type(4))) float f32x4;
typedef __attribute__((ext_vector_type(4))) unsigned short u16x4;
typedef __attribute__((ext_vector_type(8))) unsigned short u16x8;
typedef __attribute__((address_space(3))) bf16x4 lds_bf16x4;

constexpr int S_ = 2048, D_ = 576, F_ = 4608;

__device__ __forceinline__ float bf2f(u16 v) { return __uint_as_float(((unsigned)v) << 16); }
__device__ __forceinline__ u16 f2bf(float f) { __bf16 b = (__bf16)f; return __builtin_bit_cast(u16, b); }
__device__ __forceinline__ float rbf(float f) { return bf2f(f2bf(f)); }
__device__ __forceinline__ float sigmoidf_(float x) { return 1.0f / (1.0f + __expf(-x)); }

__device__ __forceinline__ void glds16s(const void* sbase, unsigned voff, unsigned lds_dst) {
  unsigned keep;
  asm volatile("s_mov_b32 %0, m0\n\ts_mov_b32 m0, %3\n\ts_nop 0\n\tglobal_load_lds_dwordx4 %1, %2\n\ts_mov_b32 m0, %0"
               : "=&s"(keep) : "v"(voff), "s"(sbase), "s"(lds_dst) : "memory");
}
__device__ __forceinline__ bf16x4 tr_read(const char* p) {
  return __builtin_amdgcn_ds_read_tr16_b64_v4i16((lds_bf16x4*)p);
}
__device__ __forceinline__ bf16x8 cat2(bf16x4 lo, bf16x4 hi) {
  return __builtin_shufflevector(lo, hi, 0, 1, 2, 3, 4, 5, 6, 7);
}
#define MFMA16(a, b, c) __builtin_amdgcn_mfma_f32_16x16x32_bf16((a), (b), (c), 0, 0, 0)
#define WAIT_V(n) asm volatile("s_waitcnt vmcnt(" #n ")" ::: "memory")

__global__ __launch_bounds__(256) void k0_ln(const u16* __restrict__ x, u16* __restrict__ xn) {
  const int tid = threadIdx.x;
  const int row = blockIdx.x * 16 + (tid >> 4), l = tid & 15;
  const u16* xr = x + (size_t)row * D_ + l * 4;
  u16x4 v[9];
#pragma unroll
  for (int j = 0; j < 9; ++j) v[j] = *(const u16x4*)(xr + j * 64);
  float sum = 0.f;
#pragma unroll
  for (int j = 0; j < 9; ++j)
#pragma unroll
    for (int e = 0; e < 4; ++e) sum += bf2f(v[j][e]);
  sum += __shfl_xor(sum, 1); sum += __shfl_xor(sum, 2); sum += __shfl_xor(sum, 4); sum += __shfl_xor(sum, 8);
  const float mean = sum * (1.0f / D_);
  float vs = 0.f;
#pragma unroll
  for (int j = 0; j < 9; ++j)
#pragma unroll
    for (int e = 0; e < 4; ++e) { float d = bf2f(v[j][e]) - mean; vs += d * d; }
  vs += __shfl_xor(vs, 1); vs += __shfl_xor(vs, 2); vs += __shfl_xor(vs, 4); vs += __shfl_xor(vs, 8);
  const float mb = rbf(mean);
  const float vb = rbf(vs * (1.0f / D_));
  const float den = rbf(sqrtf(rbf(vb + 1e-5f)));
  u16* xo = xn + (size_t)row * D_ + l * 4;
#pragma unroll
  for (int j = 0; j < 9; ++j) {
    u16x4 t;
#pragma unroll
    for (int e = 0; e < 4; ++e) t[e] = f2bf(rbf(bf2f(v[j][e]) - mb) / den);
    *(u16x4*)(xo + j * 64) = t;
  }
}

constexpr int K1_XS = 256 * 32 * 2;
constexpr int K1_GS = 32 * 144 * 2;
constexpr int K1_DS = 48 * 32 * 2;
constexpr int K1_SLOT = K1_XS + 2 * K1_GS + K1_DS;
constexpr int K1_LDS = 4 * K1_SLOT;
__device__ __forceinline__ int swz64(int row) { return (-(row >> 2)) & 3; }
__global__ __launch_bounds__(768) void k1_glu(const u16* __restrict__ xn, const u16* __restrict__ wg,
                                              const u16* __restrict__ wu, const u16* __restrict__ wre,
                                              const u16* __restrict__ wim, u16* __restrict__ h,
                                              u16* __restrict__ a, u16* __restrict__ nb) {
  extern __shared__ __attribute__((aligned(16))) char smem[];
  const int tid = threadIdx.x, lane = tid & 63, wave = __builtin_amdgcn_readfirstlane(tid >> 6);
  const int ws = wave & 3, wf = wave >> 2;
  const int xcd = blockIdx.x & 7, idx = blockIdx.x >> 3;
  const int ft = xcd * 4 + (idx >> 3), st = idx & 7;
  const int f0 = ft * 144, s0 = st * 256;
  const bool hasab = ft < 24;
  const int np = hasab ? 37 : 34;
  const u16* wdf = (ft < 12 ? wre + (size_t)ft * 48 * D_ : wim + (size_t)(hasab ? ft - 12 : 0) * 48 * D_);
  const int fr = lane & 15, fq = lane >> 4;
  unsigned voff[4];
#pragma unroll
  for (int j = 0; j < 4; ++j) {
    int i = wave + 12 * j;
    if (i < 16 || i >= 34) {
      int r = (i < 16 ? i : ((i < 37) ? i - 34 : 0)) * 16 + (lane >> 2);
      int c = (lane & 3) ^ swz64(r);
      voff[j] = (unsigned)(((i < 16) ? (s0 + r) : r) * D_ + c * 8) * 2u;
    } else {
      int jj = (i - 16) % 9;
      int sb = jj * 4 + (lane >> 4), ii = lane & 15;
      int kb = sb / 9, nbk = sb % 9;
      int r = (ii >> 1) ^ ((kb & 1) << 2);
      voff[j] = (unsigned)((kb * 8 + r) * F_ + f0 + nbk * 16 + (ii & 1) * 8) * 2u;
    }
  }
  const int P = (wave + 36 < np) ? 4 : ((wave + 24 < np) ? 3 : 2);
  const unsigned lds0 = __builtin_amdgcn_readfirstlane((unsigned)(size_t)smem);
  auto stage = [&](int kt) {
    const unsigned dst = lds0 + (kt & 3) * K1_SLOT;
#pragma unroll
    for (int j = 0; j < 4; ++j) {
      int i = wave + 12 * j;
      const u16* base = (i < 16) ? (xn + kt * 32)
                      : (i < 34) ? ((i < 25 ? wg : wu) + (size_t)kt * 32 * F_) : (wdf + kt * 32);
      if (i < np) glds16s(base, voff[j], dst + i * 1024);
    }
  };
  f32x4 accg[3][4] = {}, accu[3][4] = {}, accd[4] = {};
  const int q = (lane & 15) >> 2, p = lane & 3;
  const int sw = (fq & 1) << 2;
  stage(0); stage(1); stage(2);
#pragma unroll 1
  for (int kt = 0; kt < 18; ++kt) {
    const int rem = 17 - kt;
    const int n = P * (rem < 2 ? rem : 2);
    if (n >= 8) WAIT_V(8); else if (n >= 6) WAIT_V(6); else if (n >= 4) WAIT_V(4);
    else if (n >= 3) WAIT_V(3); else if (n >= 2) WAIT_V(2); else WAIT_V(0);
    __syncthreads();
    if (kt + 3 < 18) stage(kt + 3);
    const char* xb = smem + (kt & 3) * K1_SLOT;
    const char* wb = xb + K1_XS;
    const char* db = wb + 2 * K1_GS;
    bf16x8 xf[4], gf[3], uf[3], df;
#pragma unroll
    for (int m = 0; m < 4; ++m) {
      int r = ws * 64 + m * 16 + fr;
      xf[m] = *(const bf16x8*)(xb + r * 64 + ((fq ^ swz64(r)) * 16));
    }
#pragma unroll
    for (int t = 0; t < 3; ++t) {
      const char* bg = wb + (fq * 9 + wf * 3 + t) * 256 + p * 8;
      const char* bu = bg + K1_GS;
      gf[t] = cat2(tr_read(bg + ((q ^ sw) * 32)), tr_read(bg + (((q + 4) ^ sw) * 32)));
      uf[t] = cat2(tr_read(bu + ((q ^ sw) * 32)), tr_read(bu + (((q + 4) ^ sw) * 32)));
    }
    {
      int r = wf * 16 + fr;
      df = *(const bf16x8*)(db + r * 64 + ((fq ^ swz64(r)) * 16));
    }
#pragma unroll
    for (int t = 0; t < 3; ++t)
#pragma unroll
      for (int m = 0; m < 4; ++m) {
        accg[t][m] = MFMA16(gf[t], xf[m], accg[t][m]);
        accu[t][m] = MFMA16(uf[t], xf[m], accu[t][m]);
      }
    if (hasab) {
#pragma unroll
      for (int m = 0; m < 4; ++m) accd[m] = MFMA16(df, xf[m], accd[m]);
    }
  }
#pragma unroll
  for (int t = 0; t < 3; ++t)
#pragma unroll
    for (int m = 0; m < 4; ++m) {
      u16x4 v;
#pragma unroll
      for (int r = 0; r < 4; ++r) {
        float g = rbf(accg[t][m][r]), u = rbf(accu[t][m][r]);
        v[r] = f2bf(rbf(sigmoidf_(g)) * u);
      }
      int srow = s0 + ws * 64 + m * 16 + fr;
      *(u16x4*)(h + (size_t)srow * F_ + f0 + wf * 48 + t * 16 + fq * 4) = v;
    }
  if (hasab) {
    const bool isim = ft >= 12;
    u16* o = (isim ? nb : a) + (isim ? ft - 12 : ft) * 48 + wf * 16 + fq * 4;
    const float sg = isim ? -1.f : 1.f;
#pragma unroll
    for (int m = 0; m < 4; ++m) {
      u16x4 v;
#pragma unroll
      for (int r = 0; r < 4; ++r) v[r] = f2bf(sg * accd[m][r]);
      *(u16x4*)(o + (size_t)(s0 + ws * 64 + m * 16 + fr) * D_) = v;
    }
  }
}

constexpr int K2_BB = 128 * 144 * 2;
constexpr int K2_LDS = 4 * 36864;
#define GLOAD16(dst, voff, sbase) \
  asm volatile("global_load_dwordx4 %0, %1, %2" : "=v"(dst) : "v"(voff), "s"(sbase) : "memory")
__global__ __launch_bounds__(512) void k2_gemm(const u16* __restrict__ sre, const u16* __restrict__ sim,
                                               const u16* __restrict__ a, const u16* __restrict__ nb,
                                               const u16* __restrict__ h, const u16* __restrict__ wd,
                                               u16* __restrict__ yre, u16* __restrict__ ynim,
                                               float* __restrict__ olo, float* __restrict__ ohi) {
  extern __shared__ __attribute__((aligned(16))) char smem[];
  const int tid = threadIdx.x, lane = tid & 63, wave = __builtin_amdgcn_readfirstlane(tid >> 6);
  const int wt = wave & 1, wk = wave >> 1;
  const int xcd = blockIdx.x & 7, idx = blockIdx.x >> 3;
  const int sp = xcd >> 1;
  const int mt = (xcd & 1) * 8 + (idx >> 2), nt = idx & 3;
  const int t0 = mt * 128, e0 = nt * 144;
  const int fr = lane & 15, fq = lane >> 4;
  const u16* A; const u16* B; int lda; int nst;
  if (sp == 0) { A = sre; B = a; lda = S_; nst = 16; }
  else if (sp == 1) { A = sim; B = nb; lda = S_; nst = 16; }
  else if (sp == 2) { A = h; B = wd; lda = F_; nst = 18; }
  else { A = h + 2304; B = wd + (size_t)2304 * D_; lda = F_; nst = 18; }
  unsigned voffb[5], voffa[4];
#pragma unroll
  for (int j = 0; j < 5; ++j) {
    int i = wave + 8 * j;
    int jj = (i < 36) ? i : 0;
    int sb = jj * 4 + (lane >> 4), ii = lane & 15;
    int kb = sb / 9, nbk = sb % 9;
    int r = (ii >> 1) ^ ((kb & 1) << 2);
    voffb[j] = (unsigned)((kb * 8 + r) * D_ + e0 + nbk * 16 + (ii & 1) * 8) * 2u;
  }
#pragma unroll
  for (int m = 0; m < 4; ++m)
    voffa[m] = (unsigned)((t0 + wt * 64 + m * 16 + fr) * lda + wk * 32 + fq * 8) * 2u;
  const unsigned lds0 = __builtin_amdgcn_readfirstlane((unsigned)(size_t)smem);
  const bool p5 = wave < 4;
  f32x4 acc[9][4] = {};
  bf16x8 af0[4], af1[4], af2[4];
  const int q = (lane & 15) >> 2, p = lane & 3;
  const int sw = (fq & 1) << 2;
#define ISSUE(AF, SLOT, KT)                                                     \
  do {                                                                          \
    const u16* ab_ = A + (KT) * 128;                                            \
    _Pragma("unroll") for (int m = 0; m < 4; ++m) GLOAD16(AF[m], voffa[m], ab_); \
    const u16* bb_ = B + (size_t)(KT) * 128 * D_;                               \
    _Pragma("unroll") for (int j = 0; j < 5; ++j)                               \
      if (j < 4 || p5) glds16s(bb_, voffb[j], lds0 + (SLOT) * K2_BB + (wave + 8 * j) * 1024); \
  } while (0)
#define STEP(AF, AFN, SLOT, SLOTN, KT)                                          \
  do {                                                                          \
    if ((KT) + 1 < nst) { if (p5) WAIT_V(9); else WAIT_V(8); } else WAIT_V(0);  \
    asm volatile("" : "+v"(AF[0]), "+v"(AF[1]), "+v"(AF[2]), "+v"(AF[3]) :: "memory"); \
    __syncthreads();                                                            \
    if ((KT) + 2 < nst) ISSUE(AFN, SLOTN, (KT) + 2);                            \
    const char* bb = smem + (SLOT) * K2_BB;                                     \
    _Pragma("unroll") for (int n = 0; n < 9; ++n) {                             \
      const char* bp = bb + ((wk * 4 + fq) * 9 + n) * 256 + p * 8;              \
      bf16x8 bfr = cat2(tr_read(bp + ((q ^ sw) * 32)), tr_read(bp + (((q + 4) ^ sw) * 32))); \
      _Pragma("unroll") for (int m = 0; m < 4; ++m) acc[n][m] = MFMA16(bfr, AF[m], acc[n][m]); \
    }                                                                           \
  } while (0)
  ISSUE(af0, 0, 0);
  ISSUE(af1, 1, 1);
#pragma unroll 1
  for (int k0 = 0; k0 < nst; k0 += 3) {
    STEP(af0, af2, 0, 2, k0);
    if (k0 + 1 < nst) STEP(af1, af0, 1, 0, k0 + 1);
    if (k0 + 2 < nst) STEP(af2, af1, 2, 1, k0 + 2);
  }
  __syncthreads();
  if (wk >= 2) {
    char* dst = smem + ((wk - 2) * 2 + wt) * 36864 + lane * 16;
#pragma unroll
    for (int n = 0; n < 9; ++n)
#pragma unroll
      for (int m = 0; m < 4; ++m) *(f32x4*)(dst + (n * 4 + m) * 1024) = acc[n][m];
  }
  __syncthreads();
  if (wk < 2) {
    const char* s = smem + (wk * 2 + wt) * 36864 + lane * 16;
#pragma unroll
    for (int n = 0; n < 9; ++n)
#pragma unroll
      for (int m = 0; m < 4; ++m) acc[n][m] += *(const f32x4*)(s + (n * 4 + m) * 1024);
  }
  __syncthreads();
  if (wk == 1) {
    char* dst = smem + wt * 36864 + lane * 16;
#pragma unroll
    for (int n = 0; n < 9; ++n)
#pragma unroll
      for (int m = 0; m < 4; ++m) *(f32x4*)(dst + (n * 4 + m) * 1024) = acc[n][m];
  }
  __syncthreads();
  if (wk == 0) {
    const char* s = smem + wt * 36864 + lane * 16;
#pragma unroll
    for (int n = 0; n < 9; ++n)
#pragma unroll
      for (int m = 0; m < 4; ++m) acc[n][m] += *(const f32x4*)(s + (n * 4 + m) * 1024);
    if (sp < 2) {
      u16* o = (sp == 0 ? yre : ynim);
#pragma unroll
      for (int n = 0; n < 9; ++n)
#pragma unroll
        for (int m = 0; m < 4; ++m) {
          u16x4 v;
#pragma unroll
          for (int r = 0; r < 4; ++r) v[r] = f2bf(acc[n][m][r]);
          *(u16x4*)(o + (size_t)(t0 + wt * 64 + m * 16 + fr) * D_ + e0 + n * 16 + fq * 4) = v;
        }
    } else {
      float* o = (sp == 2 ? olo : ohi);
#pragma unroll
      for (int n = 0; n < 9; ++n)
#pragma unroll
        for (int m = 0; m < 4; ++m)
          *(f32x4*)(o + (size_t)(t0 + wt * 64 + m * 16 + fr) * D_ + e0 + n * 16 + fq * 4) = acc[n][m];
    }
  }
}

__global__ __launch_bounds__(256) void k3_combine(const u16* __restrict__ x, const u16* __restrict__ yre,
                                                  const u16* __restrict__ ynim, const float* __restrict__ olo,
                                                  const float* __restrict__ ohi, u16* __restrict__ out) {
  const size_t i = ((size_t)blockIdx.x * 256 + threadIdx.x) * 8;
  u16x8 xv = *(const u16x8*)(x + i), a = *(const u16x8*)(yre + i), b = *(const u16x8*)(ynim + i);
  f32x4 l0 = *(const f32x4*)(olo + i), l1 = *(const f32x4*)(olo + i + 4);
  f32x4 h0 = *(const f32x4*)(ohi + i), h1 = *(const f32x4*)(ohi + i + 4);
  u16x8 r;
#pragma unroll
  for (int j = 0; j < 8; ++j) {
    float s = rbf(bf2f(a[j]) + bf2f(b[j]));
    float o = rbf((j < 4 ? l0[j] : l1[j - 4]) + (j < 4 ? h0[j] : h1[j - 4]));
    float g = rbf(o * sigmoidf_(o));
    r[j] = f2bf(bf2f(xv[j]) + rbf(s * g));
  }
  *(u16x8*)(out + i) = r;
}

extern "C" void kernel_launch(void* const* d_in, const int* in_sizes, int n_in,
                              void* d_out, int out_size, void* d_ws, size_t ws_size,
                              hipStream_t stream) {
  const u16* x = (const u16*)d_in[0];
  const u16* sre = (const u16*)d_in[1];
  const u16* sim = (const u16*)d_in[2];
  const u16* wre = (const u16*)d_in[3];
  const u16* wim = (const u16*)d_in[4];
  const u16* wg = (const u16*)d_in[6];
  const u16* wu = (const u16*)d_in[7];
  const u16* wd = (const u16*)d_in[8];
  char* ws = (char*)d_ws;
  const size_t SD2 = (size_t)S_ * D_ * 2;
  u16* xn = (u16*)(ws);
  u16* a = (u16*)(ws + SD2);
  u16* nb = (u16*)(ws + 2 * SD2);
  u16* yre = (u16*)(ws + 3 * SD2);
  u16* ynim = (u16*)(ws + 4 * SD2);
  float* olo = (float*)(ws + 5 * SD2);
  float* ohi = (float*)(ws + 7 * SD2);
  u16* h = (u16*)(ws + 9 * SD2);
  (void)hipFuncSetAttribute((const void*)k1_glu, hipFuncAttributeMaxDynamicSharedMemorySize, K1_LDS);
  (void)hipFuncSetAttribute((const void*)k2_gemm, hipFuncAttributeMaxDynamicSharedMemorySize, K2_LDS);
  k0_ln<<<S_ / 16, 256, 0, stream>>>(x, xn);
  k1_glu<<<256, 768, K1_LDS, stream>>>(xn, wg, wu, wre, wim, h, a, nb);
  k2_gemm<<<256, 512, K2_LDS, stream>>>(sre, sim, a, nb, h, wd, yre, ynim, olo, ohi);
  k3_combine<<<(S_ * D_ / 8) / 256, 256, 0, stream>>>(x, yre, ynim, olo, ohi, (u16*)d_out);
}
```

```cpp
#include <hip/hip_runtime.h>
#include <hip/hip_bf16.h>

typedef unsigned short u16;
typedef __attribute__((ext_vector_type(8))) short bf16x8;
typedef __attribute__((ext_vector_type(4))) short bf16x4;
typedef __attribute__((ext_vector_type(4))) float f32x4;
typedef __attribute__((ext_vector_type(4))) unsigned short u16x4;
typedef __attribute__((ext_vector_type(8))) unsigned short u16x8;
typedef __attribute__((address_space(3))) bf16x4 lds_bf16x4;

constexpr int S_ = 2048, D_ = 576, F_ = 4608;

__device__ __forceinline__ float bf2f(u16 v) { return __uint_as_float(((unsigned)v) << 16); }
__device__ __forceinline__ u16 f2bf(float f) { __bf16 b = (__bf16)f; return __builtin_bit_cast(u16, b); }
__device__ __forceinline__ float rbf(float f) { return bf2f(f2bf(f)); }
__device__ __forceinline__ float sigmoidf_(float x) { return 1.0f / (1.0f + __expf(-x)); }

__device__ __forceinline__ void glds16s(const void* sbase, unsigned voff, unsigned lds_dst) {
  unsigned keep;
  asm volatile("s_mov_b32 %0, m0\n\ts_mov_b32 m0, %3\n\ts_nop 0\n\tglobal_load_lds_dwordx4 %1, %2\n\ts_mov_b32 m0, %0"
               : "=&s"(keep) : "v"(voff), "s"(sbase), "s"(lds_dst) : "memory");
}
__device__ __forceinline__ bf16x4 tr_read(const char* p) {
  return __builtin_amdgcn_ds_read_tr16_b64_v4i16((lds_bf16x4*)p);
}
__device__ __forceinline__ bf16x8 cat2(bf16x4 lo, bf16x4 hi) {
  return __builtin_shufflevector(lo, hi, 0, 1, 2, 3, 4, 5, 6, 7);
}
#define MFMA16(a, b, c) __builtin_amdgcn_mfma_f32_16x16x32_bf16((a), (b), (c), 0, 0, 0)
#define WAIT_V(n) asm volatile("s_waitcnt vmcnt(" #n ")" ::: "memory")
__device__ __forceinline__ int swz64(int row) { return (-(row >> 2)) & 3; }

__global__ __launch_bounds__(256) void k0_ln(const u16* __restrict__ x, u16* __restrict__ xn) {
  const int tid = threadIdx.x;
  const int row = blockIdx.x * 16 + (tid >> 4), l = tid & 15;
  const u16* xr = x + (size_t)row * D_ + l * 4;
  u16x4 v[9];
#pragma unroll
  for (int j = 0; j < 9; ++j) v[j] = *(const u16x4*)(xr + j * 64);
  float sum = 0.f;
#pragma unroll
  for (int j = 0; j < 9; ++j)
#pragma unroll
    for (int e = 0; e < 4; ++e) sum += bf2f(v[j][e]);
  sum += __shfl_xor(sum, 1); sum += __shfl_xor(sum, 2); sum += __shfl_xor(sum, 4); sum += __shfl_xor(sum, 8);
  const float mean = sum * (1.0f / D_);
  float vs = 0.f;
#pragma unroll
  for (int j = 0; j < 9; ++j)
#pragma unroll
    for (int e = 0; e < 4; ++e) { float d = bf2f(v[j][e]) - mean; vs += d * d; }
  vs += __shfl_xor(vs, 1); vs += __shfl_xor(vs, 2); vs += __shfl_xor(vs, 4); vs += __shfl_xor(vs, 8);
  const float mb = rbf(mean);
  const float vb = rbf(vs * (1.0f / D_));
  const float den = rbf(sqrtf(rbf(vb + 1e-5f)));
  const int r = row & 15;
  char* xo = (char*)xn + (size_t)(row >> 4) * 18 * 1024 + r * 64;
#pragma unroll
  for (int j = 0; j < 9; ++j) {
    u16x4 t;
#pragma unroll
    for (int e = 0; e < 4; ++e) t[e] = f2bf(rbf(bf2f(v[j][e]) - mb) / den);
    int col = j * 64 + l * 4;
    int kb = col >> 5, c = (col & 31) >> 3;
    *(u16x4*)(xo + kb * 1024 + ((c ^ swz64(r)) * 16) + (col & 7) * 2) = t;
  }
}

constexpr int K1_XS = 256 * 32 * 2;
constexpr int K1_GS = 32 * 144 * 2;
constexpr int K1_DS = 48 * 32 * 2;
constexpr int K1_SLOT = K1_XS + 2 * K1_GS + K1_DS;
constexpr int K1_LDS = 4 * K1_SLOT;
__global__ __launch_bounds__(768) void k1_glu(const u16* __restrict__ xn, const u16* __restrict__ wg,
                                              const u16* __restrict__ wu, const u16* __restrict__ wre,
                                              const u16* __restrict__ wim, u16* __restrict__ h,
                                              u16* __restrict__ a, u16* __restrict__ nb) {
  extern __shared__ __attribute__((aligned(16))) char smem[];
  const int tid = threadIdx.x, lane = tid & 63, wave = __builtin_amdgcn_readfirstlane(tid >> 6);
  const int ws = wave & 3, wf = wave >> 2;
  const int xcd = blockIdx.x & 7, idx = blockIdx.x >> 3;
  const int ft = xcd * 4 + (idx >> 3), st = idx & 7;
  const int f0 = ft * 144, s0 = st * 256;
  const bool hasab = ft < 24;
  const int np = hasab ? 37 : 34;
  const u16* wdf = (ft < 12 ? wre + (size_t)ft * 48 * D_ : wim + (size_t)(hasab ? ft - 12 : 0) * 48 * D_);
  const int fr = lane & 15, fq = lane >> 4;
  unsigned voff[4];
#pragma unroll
  for (int j = 0; j < 4; ++j) {
    int i = wave + 12 * j;
    if (i < 16) {
      voff[j] = (unsigned)((s0 / 16 + i) * 18 * 1024 + lane * 16);
    } else if (i >= 34) {
      int r = ((i < 37) ? i - 34 : 0) * 16 + (lane >> 2);
      int c = (lane & 3) ^ swz64(r);
      voff[j] = (unsigned)(r * D_ + c * 8) * 2u;
    } else {
      int jj = (i - 16) % 9;
      int sb = jj * 4 + (lane >> 4), ii = lane & 15;
      int kb = sb / 9, nbk = sb % 9;
      int r = (ii >> 1) ^ ((kb & 1) << 2);
      voff[j] = (unsigned)((kb * 8 + r) * F_ + f0 + nbk * 16 + (ii & 1) * 8) * 2u;
    }
  }
  const int P = (wave + 36 < np) ? 4 : ((wave + 24 < np) ? 3 : 2);
  const unsigned lds0 = __builtin_amdgcn_readfirstlane((unsigned)(size_t)smem);
  auto piece = [&](int kt, int j) {
    const unsigned dst = lds0 + (kt & 3) * K1_SLOT;
    int i = wave + 12 * j;
    const u16* base = (i < 16) ? (xn + kt * 512)
                    : (i < 34) ? ((i < 25 ? wg : wu) + (size_t)kt * 32 * F_) : (wdf + kt * 32);
    if (i < np) glds16s(base, voff[j], dst + i * 1024);
  };
  auto stage = [&](int kt) {
#pragma unroll
    for (int j = 0; j < 4; ++j) piece(kt, j);
  };
  f32x4 accg[3][4] = {}, accu[3][4] = {}, accd[4] = {};
  const int q = (lane & 15) >> 2, p = lane & 3;
  const int sw = (fq & 1) << 2;
  stage(0); stage(1); stage(2);
#pragma unroll 1
  for (int kt = 0; kt < 18; ++kt) {
    const int rem = 17 - kt;
    const int n = P * (rem < 2 ? rem : 2);
    if (n >= 8) WAIT_V(8); else if (n >= 6) WAIT_V(6); else if (n >= 4) WAIT_V(4);
    else if (n >= 3) WAIT_V(3); else if (n >= 2) WAIT_V(2); else WAIT_V(0);
    __syncthreads();
    const bool pf = kt + 3 < 18;
    const char* xb = smem + (kt & 3) * K1_SLOT;
    const char* wb = xb + K1_XS;
    const char* db = wb + 2 * K1_GS;
    bf16x8 xf[4], gf[3], uf[3], df;
#pragma unroll
    for (int m = 0; m < 4; ++m) {
      int r = ws * 64 + m * 16 + fr;
      xf[m] = *(const bf16x8*)(xb + r * 64 + ((fq ^ swz64(r)) * 16));
    }
#pragma unroll
    for (int t = 0; t < 3; ++t) {
      const char* bg = wb + (fq * 9 + wf * 3 + t) * 256 + p * 8;
      const char* bu = bg + K1_GS;
      gf[t] = cat2(tr_read(bg + ((q ^ sw) * 32)), tr_read(bg + (((q + 4) ^ sw) * 32)));
      uf[t] = cat2(tr_read(bu + ((q ^ sw) * 32)), tr_read(bu + (((q + 4) ^ sw) * 32)));
    }
    {
      int r = wf * 16 + fr;
      df = *(const bf16x8*)(db + r * 64 + ((fq ^ swz64(r)) * 16));
    }
#pragma unroll
    for (int t = 0; t < 3; ++t) {
#pragma unroll
      for (int m = 0; m < 4; ++m) {
        accg[t][m] = MFMA16(gf[t], xf[m], accg[t][m]);
        accu[t][m] = MFMA16(uf[t], xf[m], accu[t][m]);
      }
      __builtin_amdgcn_sched_barrier(0);
      if (pf) piece(kt + 3, t);
      __builtin_amdgcn_sched_barrier(0);
    }
    if (hasab) {
#pragma unroll
      for (int m = 0; m < 4; ++m) accd[m] = MFMA16(df, xf[m], accd[m]);
    }
    __builtin_amdgcn_sched_barrier(0);
    if (pf) piece(kt + 3, 3);
  }
#pragma unroll
  for (int t = 0; t < 3; ++t)
#pragma unroll
    for (int m = 0; m < 4; ++m) {
      u16x4 v;
#pragma unroll
      for (int r = 0; r < 4; ++r) {
        float g = rbf(accg[t][m][r]), u = rbf(accu[t][m][r]);
        v[r] = f2bf(rbf(sigmoidf_(g)) * u);
      }
      int srow = s0 + ws * 64 + m * 16 + fr;
      *(u16x4*)(h + (size_t)srow * F_ + f0 + wf * 48 + t * 16 + fq * 4) = v;
    }
  if (hasab) {
    const bool isim = ft >= 12;
    const int e = (isim ? ft - 12 : ft) * 48 + wf * 16;
    const int nt = e / 144, nbk = (e % 144) >> 4;
    char* o = (char*)(isim ? nb : a) + (size_t)nt * 32 * 18432 + nbk * 256 + fq * 8;
    const float sg = isim ? -1.f : 1.f;
#pragma unroll
    for (int m = 0; m < 4; ++m) {
      u16x4 v;
#pragma unroll
      for (int r = 0; r < 4; ++r) v[r] = f2bf(sg * accd[m][r]);
      int s = s0 + ws * 64 + m * 16 + fr;
      int stg = s >> 6, kb = (s & 63) >> 3, rr = (s & 7) ^ ((kb & 1) << 2);
      *(u16x4*)(o + (size_t)stg * 18432 + kb * 2304 + rr * 32) = v;
    }
  }
}

constexpr int K2_AS = 128 * 64 * 2;
constexpr int K2_BS = 64 * 144 * 2;
constexpr int K2_SLOT = K2_AS + K2_BS;
constexpr int K2_LDS = 4 * K2_SLOT;
__global__ __launch_bounds__(512) void k2_gemm(const u16* __restrict__ sre, const u16* __restrict__ sim,
                                               const u16* __restrict__ a, const u16* __restrict__ nb,
                                               const u16* __restrict__ h, const u16* __restrict__ wd,
                                               u16* __restrict__ yre, u16* __restrict__ ynim,
                                               float* __restrict__ olo, float* __restrict__ ohi) {
  extern __shared__ __attribute__((aligned(16))) char smem[];
  const int tid = threadIdx.x, lane = tid & 63, wave = __builtin_amdgcn_readfirstlane(tid >> 6);
  const int wt = wave & 3, wk = wave >> 2;
  const int xcd = blockIdx.x & 7, idx = blockIdx.x >> 3;
  const int sp = xcd >> 1;
  const int mt = (xcd & 1) * 8 + (idx >> 2), nt = idx & 3;
  const int t0 = mt * 128, e0 = nt * 144;
  const int fr = lane & 15, fq = lane >> 4;
  const u16* A; const u16* B; int lda; int nst; int bstep;
  const bool img = sp < 2;
  if (sp == 0) { A = sre; B = a + (size_t)nt * 32 * 9216; lda = S_; nst = 32; bstep = 9216; }
  else if (sp == 1) { A = sim; B = nb + (size_t)nt * 32 * 9216; lda = S_; nst = 32; bstep = 9216; }
  else if (sp == 2) { A = h; B = wd; lda = F_; nst = 36; bstep = 64 * D_; }
  else { A = h + 2304; B = wd + (size_t)2304 * D_; lda = F_; nst = 36; bstep = 64 * D_; }
  unsigned voff[5];
#pragma unroll
  for (int j = 0; j < 5; ++j) {
    int i = wave + 8 * j;
    if (i < 16) {
      int r = i * 8 + (lane >> 3);
      int c = (lane & 7) ^ (lane >> 3);
      voff[j] = (unsigned)((t0 + r) * lda + c * 8) * 2u;
    } else {
      int jj = (i < 34) ? (i - 16) : 0;
      if (img) {
        voff[j] = (unsigned)(jj * 1024 + lane * 16);
      } else {
        int sb = jj * 4 + (lane >> 4), ii = lane & 15;
        int kb = sb / 9, nbk = sb % 9;
        int r = (ii >> 1) ^ ((kb & 1) << 2);
        voff[j] = (unsigned)((kb * 8 + r) * D_ + e0 + nbk * 16 + (ii & 1) * 8) * 2u;
      }
    }
  }
  const bool p5 = wave < 2;
  const unsigned lds0 = __builtin_amdgcn_readfirstlane((unsigned)(size_t)smem);
  auto piece = [&](int kt, int j) {
    const u16* base = (j < 2) ? (A + kt * 64) : (B + (size_t)kt * bstep);
    if (j < 4 || p5) glds16s(base, voff[j], lds0 + (kt & 3) * K2_SLOT + (wave + 8 * j) * 1024);
  };
  f32x4 acc[9][2] = {};
  const int q = (lane & 15) >> 2, p = lane & 3;
  const int sw = (fq & 1) << 2;
#pragma unroll
  for (int s = 0; s < 3; ++s)
#pragma unroll
    for (int j = 0; j < 5; ++j) piece(s, j);
#pragma unroll 1
  for (int kt = 0; kt < nst; ++kt) {
    const int rem = nst - 1 - kt;
    if (rem >= 2) { if (p5) WAIT_V(10); else WAIT_V(8); }
    else if (rem == 1) { if (p5) WAIT_V(5); else WAIT_V(4); }
    else WAIT_V(0);
    __syncthreads();
    const bool pf = kt + 3 < nst;
    const char* ab = smem + (kt & 3) * K2_SLOT;
    const char* bb = ab + K2_AS;
    bf16x8 af[2], bfr[9];
#pragma unroll
    for (int m = 0; m < 2; ++m) {
      int r = wt * 32 + m * 16 + fr;
      int c = wk * 4 + fq;
      af[m] = *(const bf16x8*)(ab + r * 128 + ((c ^ (r & 7)) * 16));
    }
#pragma unroll
    for (int n = 0; n < 9; ++n) {
      const char* bp = bb + ((wk * 4 + fq) * 9 + n) * 256 + p * 8;
      bfr[n] = cat2(tr_read(bp + ((q ^ sw) * 32)), tr_read(bp + (((q + 4) ^ sw) * 32)));
    }
#pragma unroll
    for (int n = 0; n < 9; ++n) {
#pragma unroll
      for (int m = 0; m < 2; ++m) acc[n][m] = MFMA16(bfr[n], af[m], acc[n][m]);
      if ((n & 1) == 0) {
        __builtin_amdgcn_sched_barrier(0);
        if (pf) piece(kt + 3, n >> 1);
        __builtin_amdgcn_sched_barrier(0);
      }
    }
  }
  __syncthreads();
  {
    char* dst = smem + wave * 10240 + lane * 16;
#pragma unroll
    for (int n = 0; n < 9; ++n)
#pragma unroll
      for (int m = 0; m < 2; ++m) {
        bool mine = (wk == 0) ? (n < 5) : (n >= 5);
        int slot = (wk == 0) ? (n - 5) : n;
        if (!mine) *(f32x4*)(dst + (slot * 2 + m) * 1024) = acc[n][m];
      }
  }
  __syncthreads();
  {
    const char* s = smem + (wave ^ 4) * 10240 + lane * 16;
#pragma unroll
    for (int n = 0; n < 9; ++n)
#pragma unroll
      for (int m = 0; m < 2; ++m) {
        bool mine = (wk == 0) ? (n < 5) : (n >= 5);
        int slot = (wk == 0) ? n : (n - 5);
        if (mine) {
          f32x4 v = acc[n][m] + *(const f32x4*)(s + (slot * 2 + m) * 1024);
          size_t off = (size_t)(t0 + wt * 32 + m * 16 + fr) * D_ + e0 + n * 16 + fq * 4;
          if (sp < 2) {
            u16x4 w;
#pragma unroll
            for (int r = 0; r < 4; ++r) w[r] = f2bf(v[r]);
            *(u16x4*)((sp == 0 ? yre : ynim) + off) = w;
          } else {
            *(f32x4*)((sp == 2 ? olo : ohi) + off) = v;
          }
        }
      }
  }
}

__global__ __launch_bounds__(256) void k3_combine(const u16* __restrict__ x, const u16* __restrict__ yre,
                                                  const u16* __restrict__ ynim, const float* __restrict__ olo,
                                                  const float* __restrict__ ohi, u16* __restrict__ out) {
  const size_t i = ((size_t)blockIdx.x * 256 + threadIdx.x) * 8;
  u16x8 xv = *(const u16x8*)(x + i), a = *(const u16x8*)(yre + i), b = *(const u16x8*)(ynim + i);
  f32x4 l0 = *(const f32x4*)(olo + i), l1 = *(const f32x4*)(olo + i + 4);
  f32x4 h0 = *(const f32x4*)(ohi + i), h1 = *(const f32x4*)(ohi + i + 4);
  u16x8 r;
#pragma unroll
  for (int j = 0; j < 8; ++j) {
    float s = rbf(bf2f(a[j]) + bf2f(b[j]));
    float o = rbf((j < 4 ? l0[j] : l1[j - 4]) + (j < 4 ? h0[j] : h1[j - 4]));
    float g = rbf(o * sigmoidf_(o));
    r[j] = f2bf(bf2f(xv[j]) + rbf(s * g));
  }
  *(u16x8*)(out + i) = r;
}

extern "C" void kernel_launch(void* const* d_in, const int* in_sizes, int n_in,
                              void* d_out, int out_size, void* d_ws, size_t ws_size,
                              hipStream_t stream) {
  const u16* x = (const u16*)d_in[0];
  const u16* sre = (const u16*)d_in[1];
  const u16* sim = (const u16*)d_in[2];
  const u16* wre = (const u16*)d_in[3];
  const u16* wim = (const u16*)d_in[4];
  const u16* wg = (const u16*)d_in[6];
  const u16* wu = (const u16*)d_in[7];
  const u16* wd = (const u16*)d_in[8];
  char* ws = (char*)d_ws;
  const size_t SD2 = (size_t)S_ * D_ * 2;
  u16* xn = (u16*)(ws);
  u16* a = (u16*)(ws + SD2);
  u16* nb = (u16*)(ws + 2 * SD2);
  u16* yre = (u16*)(ws + 3 * SD2);
  u16* ynim = (u16*)(ws + 4 * SD2);
  float* olo = (float*)(ws + 5 * SD2);
  float* ohi = (float*)(ws + 7 * SD2);
  u16* h = (u16*)(ws + 9 * SD2);
  (void)hipFuncSetAttribute((const void*)k1_glu, hipFuncAttributeMaxDynamicSharedMemorySize, K1_LDS);
  (void)hipFuncSetAttribute((const void*)k2_gemm, hipFuncAttributeMaxDynamicSharedMemorySize, K2_LDS);
  k0_ln<<<S_ / 16, 256, 0, stream>>>(x, xn);
  k1_glu<<<256, 768, K1_LDS, stream>>>(xn, wg, wu, wre, wim, h, a, nb);
  k2_gemm<<<256, 512, K2_LDS, stream>>>(sre, sim, a, nb, h, wd, yre, ynim, olo, ohi);
  k3_combine<<<(S_ * D_ / 8) / 256, 256, 0, stream>>>(x, yre, ynim, olo, ohi, (u16*)d_out);
}
```

```cpp
#include <hip/hip_runtime.h>
#include <hip/hip_bf16.h>

typedef unsigned short u16;
typedef __attribute__((ext_vector_type(8))) short bf16x8;
typedef __attribute__((ext_vector_type(4))) short bf16x4;
typedef __attribute__((ext_vector_type(4))) float f32x4;
typedef __attribute__((ext_vector_type(4))) unsigned short u16x4;
typedef __attribute__((ext_vector_type(8))) unsigned short u16x8;
typedef __attribute__((address_space(3))) bf16x4 lds_bf16x4;

constexpr int S_ = 2048, D_ = 576, F_ = 4608;

__device__ __forceinline__ float bf2f(u16 v) { return __uint_as_float(((unsigned)v) << 16); }
__device__ __forceinline__ u16 f2bf(float f) { __bf16 b = (__bf16)f; return __builtin_bit_cast(u16, b); }
__device__ __forceinline__ float rbf(float f) { return bf2f(f2bf(f)); }
__device__ __forceinline__ float sigmoidf_(float x) { return 1.0f / (1.0f + __expf(-x)); }

__device__ __forceinline__ void glds16s(const void* sbase, unsigned voff, unsigned lds_dst) {
  unsigned keep;
  asm volatile("s_mov_b32 %0, m0\n\ts_mov_b32 m0, %3\n\ts_nop 0\n\tglobal_load_lds_dwordx4 %1, %2\n\ts_mov_b32 m0, %0"
               : "=&s"(keep) : "v"(voff), "s"(sbase), "s"(lds_dst) : "memory");
}
__device__ __forceinline__ bf16x4 tr_read(const char* p) {
  return __builtin_amdgcn_ds_read_tr16_b64_v4i16((lds_bf16x4*)p);
}
__device__ __forceinline__ bf16x8 cat2(bf16x4 lo, bf16x4 hi) {
  return __builtin_shufflevector(lo, hi, 0, 1, 2, 3, 4, 5, 6, 7);
}
#define MFMA16(a, b, c) __builtin_amdgcn_mfma_f32_16x16x32_bf16((a), (b), (c), 0, 0, 0)
#define WAIT_V(n) asm volatile("s_waitcnt vmcnt(" #n ")" ::: "memory")
__device__ __forceinline__ int swz64(int row) { return (-(row >> 2)) & 3; }

__global__ __launch_bounds__(256) void k0_ln(const u16* __restrict__ x, u16* __restrict__ xn) {
  const int tid = threadIdx.x;
  const int row = blockIdx.x * 16 + (tid >> 4), l = tid & 15;
  const u16* xr = x + (size_t)row * D_ + l * 4;
  u16x4 v[9];
#pragma unroll
  for (int j = 0; j < 9; ++j) v[j] = *(const u16x4*)(xr + j * 64);
  float sum = 0.f;
#pragma unroll
  for (int j = 0; j < 9; ++j)
#pragma unroll
    for (int e = 0; e < 4; ++e) sum += bf2f(v[j][e]);
  sum += __shfl_xor(sum, 1); sum += __shfl_xor(sum, 2); sum += __shfl_xor(sum, 4); sum += __shfl_xor(sum, 8);
  const float mean = sum * (1.0f / D_);
  float vs = 0.f;
#pragma unroll
  for (int j = 0; j < 9; ++j)
#pragma unroll
    for (int e = 0; e < 4; ++e) { float d = bf2f(v[j][e]) - mean; vs += d * d; }
  vs += __shfl_xor(vs, 1); vs += __shfl_xor(vs, 2); vs += __shfl_xor(vs, 4); vs += __shfl_xor(vs, 8);
  const float mb = rbf(mean);
  const float vb = rbf(vs * (1.0f / D_));
  const float den = rbf(sqrtf(rbf(vb + 1e-5f)));
  const int r = row & 15;
  char* xo = (char*)xn + (size_t)(row >> 4) * 18 * 1024 + r * 64;
#pragma unroll
  for (int j = 0; j < 9; ++j) {
    u16x4 t;
#pragma unroll
    for (int e = 0; e < 4; ++e) t[e] = f2bf(rbf(bf2f(v[j][e]) - mb) / den);
    int col = j * 64 + l * 4;
    int kb = col >> 5, c = (col & 31) >> 3;
    *(u16x4*)(xo + kb * 1024 + ((c ^ swz64(r)) * 16) + (col & 7) * 2) = t;
  }
}

constexpr int K1_XS = 256 * 32 * 2;
constexpr int K1_GS = 32 * 144 * 2;
constexpr int K1_DS = 48 * 32 * 2;
constexpr int K1_SLOT = K1_XS + 2 * K1_GS + K1_DS;
constexpr int K1_LDS = 4 * K1_SLOT;
__global__ __launch_bounds__(768) void k1_glu(const u16* __restrict__ xn, const u16* __restrict__ wg,
                                              const u16* __restrict__ wu, const u16* __restrict__ wre,
                                              const u16* __restrict__ wim, u16* __restrict__ h,
                                              u16* __restrict__ a, u16* __restrict__ nb) {
  extern __shared__ __attribute__((aligned(16))) char smem[];
  const int tid = threadIdx.x, lane = tid & 63, wave = __builtin_amdgcn_readfirstlane(tid >> 6);
  const int ws = wave & 3, wf = wave >> 2;
  const int xcd = blockIdx.x & 7, idx = blockIdx.x >> 3;
  const int ft = xcd * 4 + (idx >> 3), st = idx & 7;
  const int f0 = ft * 144, s0 = st * 256;
  const bool hasab = ft < 24;
  const int np = hasab ? 37 : 34;
  const u16* wdf = (ft < 12 ? wre + (size_t)ft * 48 * D_ : wim + (size_t)(hasab ? ft - 12 : 0) * 48 * D_);
  const int fr = lane & 15, fq = lane >> 4;
  unsigned voff[4];
#pragma unroll
  for (int j = 0; j < 4; ++j) {
    int i = wave + 12 * j;
    if (i < 16) {
      voff[j] = (unsigned)((s0 / 16 + i) * 18 * 1024 + lane * 16);
    } else if (i >= 34) {
      int r = ((i < 37) ? i - 34 : 0) * 16 + (lane >> 2);
      int c = (lane & 3) ^ swz64(r);
      voff[j] = (unsigned)(r * D_ + c * 8) * 2u;
    } else {
      int jj = (i - 16) % 9;
      int sb = jj * 4 + (lane >> 4), ii = lane & 15;
      int kb = sb / 9, nbk = sb % 9;
      int r = (ii >> 1) ^ ((kb & 1) << 2);
      voff[j] = (unsigned)((kb * 8 + r) * F_ + f0 + nbk * 16 + (ii & 1) * 8) * 2u;
    }
  }
  const int P = (wave + 36 < np) ? 4 : ((wave + 24 < np) ? 3 : 2);
  const unsigned lds0 = __builtin_amdgcn_readfirstlane((unsigned)(size_t)smem);
  auto piece = [&](int kt, int j) {
    const unsigned dst = lds0 + (kt & 3) * K1_SLOT;
    int i = wave + 12 * j;
    const u16* base = (i < 16) ? (xn + kt * 512)
                    : (i < 34) ? ((i < 25 ? wg : wu) + (size_t)kt * 32 * F_) : (wdf + kt * 32);
    if (i < np) glds16s(base, voff[j], dst + i * 1024);
  };
  auto stage = [&](int kt) {
#pragma unroll
    for (int j = 0; j < 4; ++j) piece(kt, j);
  };
  f32x4 accg[3][4] = {}, accu[3][4] = {}, accd[4] = {};
  const int q = (lane & 15) >> 2, p = lane & 3;
  const int sw = (fq & 1) << 2;
  const bool g1 = wf == 2;
  auto waitn = [&](int n) {
    if (n >= 12) WAIT_V(12); else if (n >= 9) WAIT_V(9); else if (n >= 8) WAIT_V(8); else if (n >= 6) WAIT_V(6);
    else if (n >= 4) WAIT_V(4); else if (n >= 3) WAIT_V(3); else if (n >= 2) WAIT_V(2); else WAIT_V(0);
  };
  bf16x8 xf[4], gf[2], uf[2];
  auto rdgu = [&](int kt, int t, bf16x8& g, bf16x8& u) {
    const char* bg = smem + (kt & 3) * K1_SLOT + K1_XS + (fq * 9 + wf * 3 + t) * 256 + p * 8;
    const char* bu = bg + K1_GS;
    g = cat2(tr_read(bg + ((q ^ sw) * 32)), tr_read(bg + (((q + 4) ^ sw) * 32)));
    u = cat2(tr_read(bu + ((q ^ sw) * 32)), tr_read(bu + (((q + 4) ^ sw) * 32)));
  };
  auto readf = [&](int kt) {
    const char* xb = smem + (kt & 3) * K1_SLOT;
#pragma unroll
    for (int m = 0; m < 4; ++m) {
      int r = ws * 64 + m * 16 + fr;
      xf[m] = *(const bf16x8*)(xb + r * 64 + ((fq ^ swz64(r)) * 16));
    }
    rdgu(kt, 0, gf[0], uf[0]);
    rdgu(kt, 1, gf[1], uf[1]);
  };
  auto mma = [&](int kt) {
    const int pkt = kt + 3;
    const bool pf = pkt < 18;
#pragma unroll
    for (int m = 0; m < 4; ++m) {
      accg[0][m] = MFMA16(gf[0], xf[m], accg[0][m]);
      accu[0][m] = MFMA16(uf[0], xf[m], accu[0][m]);
    }
    __builtin_amdgcn_sched_barrier(0);
    bf16x8 g2, u2, df;
    rdgu(kt, 2, g2, u2);
    {
      int r = wf * 16 + fr;
      df = *(const bf16x8*)(smem + (kt & 3) * K1_SLOT + K1_XS + 2 * K1_GS + r * 64 + ((fq ^ swz64(r)) * 16));
    }
    if (pf) piece(pkt, 0);
    __builtin_amdgcn_sched_barrier(0);
#pragma unroll
    for (int m = 0; m < 4; ++m) {
      accg[1][m] = MFMA16(gf[1], xf[m], accg[1][m]);
      accu[1][m] = MFMA16(uf[1], xf[m], accu[1][m]);
    }
    __builtin_amdgcn_sched_barrier(0);
    if (pf) piece(pkt, 1);
    __builtin_amdgcn_sched_barrier(0);
#pragma unroll
    for (int m = 0; m < 4; ++m) {
      accg[2][m] = MFMA16(g2, xf[m], accg[2][m]);
      accu[2][m] = MFMA16(u2, xf[m], accu[2][m]);
    }
    __builtin_amdgcn_sched_barrier(0);
    if (pf) piece(pkt, 2);
    __builtin_amdgcn_sched_barrier(0);
    if (hasab) {
#pragma unroll
      for (int m = 0; m < 4; ++m) accd[m] = MFMA16(df, xf[m], accd[m]);
    }
    __builtin_amdgcn_sched_barrier(0);
    if (pf) piece(pkt, 3);
  };
  stage(0); stage(1); stage(2);
  waitn(2 * P);
  __syncthreads();
  if (!g1) {
#pragma unroll 1
    for (int kt = 0; kt < 18; ++kt) {
      readf(kt);
      __syncthreads();
      mma(kt);
      const int rem = 16 - kt;
      waitn(rem <= 0 ? 0 : P * (rem < 2 ? rem : 2));
      __syncthreads();
    }
    __syncthreads();
  } else {
    __syncthreads();
#pragma unroll 1
    for (int kt = 0; kt < 18; ++kt) {
      readf(kt);
      const int rem = 16 - kt;
      waitn(rem <= 0 ? 0 : P);
      __syncthreads();
      mma(kt);
      __syncthreads();
    }
  }
#pragma unroll
  for (int t = 0; t < 3; ++t)
#pragma unroll
    for (int m = 0; m < 4; ++m) {
      u16x4 v;
#pragma unroll
      for (int r = 0; r < 4; ++r) {
        float g = rbf(accg[t][m][r]), u = rbf(accu[t][m][r]);
        v[r] = f2bf(rbf(sigmoidf_(g)) * u);
      }
      int srow = s0 + ws * 64 + m * 16 + fr;
      *(u16x4*)(h + (size_t)srow * F_ + f0 + wf * 48 + t * 16 + fq * 4) = v;
    }
  if (hasab) {
    const bool isim = ft >= 12;
    const int e = (isim ? ft - 12 : ft) * 48 + wf * 16;
    const int nt = e / 144, nbk = (e % 144) >> 4;
    char* o = (char*)(isim ? nb : a) + (size_t)nt * 32 * 18432 + nbk * 256 + fq * 8;
    const float sg = isim ? -1.f : 1.f;
#pragma unroll
    for (int m = 0; m < 4; ++m) {
      u16x4 v;
#pragma unroll
      for (int r = 0; r < 4; ++r) v[r] = f2bf(sg * accd[m][r]);
      int s = s0 + ws * 64 + m * 16 + fr;
      int stg = s >> 6, kb = (s & 63) >> 3, rr = (s & 7) ^ ((kb & 1) << 2);
      *(u16x4*)(o + (size_t)stg * 18432 + kb * 2304 + rr * 32) = v;
    }
  }
}

constexpr int K2_AS = 128 * 64 * 2;
constexpr int K2_BS = 64 * 144 * 2;
constexpr int K2_SLOT = K2_AS + K2_BS;
constexpr int K2_LDS = 4 * K2_SLOT;
__global__ __launch_bounds__(512) void k2_gemm(const u16* __restrict__ sre, const u16* __restrict__ sim,
                                               const u16* __restrict__ a, const u16* __restrict__ nb,
                                               const u16* __restrict__ h, const u16* __restrict__ wd,
                                               u16* __restrict__ yre, u16* __restrict__ ynim,
                                               float* __restrict__ olo, float* __restrict__ ohi) {
  extern __shared__ __attribute__((aligned(16))) char smem[];
  const int tid = threadIdx.x, lane = tid & 63, wave = __builtin_amdgcn_readfirstlane(tid >> 6);
  const int wt = wave & 3, wk = wave >> 2;
  const int xcd = blockIdx.x & 7, idx = blockIdx.x >> 3;
  const int sp = xcd >> 1;
  const int mt = (xcd & 1) * 8 + (idx >> 2), nt = idx & 3;
  const int t0 = mt * 128, e0 = nt * 144;
  const int fr = lane & 15, fq = lane >> 4;
  const u16* A; const u16* B; int lda; int nst; int bstep;
  const bool img = sp < 2;
  if (sp == 0) { A = sre; B = a + (size_t)nt * 32 * 9216; lda = S_; nst = 32; bstep = 9216; }
  else if (sp == 1) { A = sim; B = nb + (size_t)nt * 32 * 9216; lda = S_; nst = 32; bstep = 9216; }
  else if (sp == 2) { A = h; B = wd; lda = F_; nst = 36; bstep = 64 * D_; }
  else { A = h + 2304; B = wd + (size_t)2304 * D_; lda = F_; nst = 36; bstep = 64 * D_; }
  unsigned voff[5];
#pragma unroll
  for (int j = 0; j < 5; ++j) {
    int i = wave + 8 * j;
    if (i < 16) {
      int r = i * 8 + (lane >> 3);
      int c = (lane & 7) ^ (lane >> 3);
      voff[j] = (unsigned)((t0 + r) * lda + c * 8) * 2u;
    } else {
      int jj = (i < 34) ? (i - 16) : 0;
      if (img) {
        voff[j] = (unsigned)(jj * 1024 + lane * 16);
      } else {
        int sb = jj * 4 + (lane >> 4), ii = lane & 15;
        int kb = sb / 9, nbk = sb % 9;
        int r = (ii >> 1) ^ ((kb & 1) << 2);
        voff[j] = (unsigned)((kb * 8 + r) * D_ + e0 + nbk * 16 + (ii & 1) * 8) * 2u;
      }
    }
  }
  const bool p5 = wave < 2;
  const unsigned lds0 = __builtin_amdgcn_readfirstlane((unsigned)(size_t)smem);
  auto piece = [&](int kt, int j) {
    const u16* base = (j < 2) ? (A + kt * 64) : (B + (size_t)kt * bstep);
    if (j < 4 || p5) glds16s(base, voff[j], lds0 + (kt & 3) * K2_SLOT + (wave + 8 * j) * 1024);
  };
  f32x4 acc[9][2] = {};
  const int q = (lane & 15) >> 2, p = lane & 3;
  const int sw = (fq & 1) << 2;
  const bool g1 = wk == 1;
  const int P = p5 ? 5 : 4;
  auto waitn = [&](int n) {
    if (n >= 15) WAIT_V(15); else if (n >= 12) WAIT_V(12); else if (n >= 10) WAIT_V(10); else if (n >= 8) WAIT_V(8);
    else if (n >= 5) WAIT_V(5); else if (n >= 4) WAIT_V(4); else WAIT_V(0);
  };
  bf16x8 af[2], bfr[9];
  auto readf = [&](int kt) {
    const char* ab = smem + (kt & 3) * K2_SLOT;
    const char* bb = ab + K2_AS;
#pragma unroll
    for (int m = 0; m < 2; ++m) {
      int r = wt * 32 + m * 16 + fr;
      int c = wk * 4 + fq;
      af[m] = *(const bf16x8*)(ab + r * 128 + ((c ^ (r & 7)) * 16));
    }
#pragma unroll
    for (int n = 0; n < 9; ++n) {
      const char* bp = bb + ((wk * 4 + fq) * 9 + n) * 256 + p * 8;
      bfr[n] = cat2(tr_read(bp + ((q ^ sw) * 32)), tr_read(bp + (((q + 4) ^ sw) * 32)));
    }
  };
  auto mma = [&](int pkt) {
    const bool pf = pkt < nst;
#pragma unroll
    for (int n = 0; n < 9; ++n) {
#pragma unroll
      for (int m = 0; m < 2; ++m) acc[n][m] = MFMA16(bfr[n], af[m], acc[n][m]);
      if ((n & 1) == 0) {
        __builtin_amdgcn_sched_barrier(0);
        if (pf) piece(pkt, n >> 1);
        __builtin_amdgcn_sched_barrier(0);
      }
    }
  };
#pragma unroll
  for (int s = 0; s < 3; ++s)
#pragma unroll
    for (int j = 0; j < 5; ++j) piece(s, j);
  if (g1) {
#pragma unroll
    for (int j = 0; j < 5; ++j) piece(3, j);
  }
  waitn(g1 ? 3 * P : 2 * P);
  __syncthreads();
  if (!g1) {
#pragma unroll 1
    for (int kt = 0; kt < nst; ++kt) {
      readf(kt);
      __syncthreads();
      mma(kt + 3);
      const int rem = nst - 2 - kt;
      waitn(rem <= 0 ? 0 : P * (rem < 2 ? rem : 2));
      __syncthreads();
    }
    __syncthreads();
  } else {
    __syncthreads();
#pragma unroll 1
    for (int kt = 0; kt < nst; ++kt) {
      readf(kt);
      const int rem = nst - 2 - kt;
      waitn(rem <= 0 ? 0 : P * (rem < 2 ? rem : 2));
      __syncthreads();
      mma(kt + 4);
      __syncthreads();
    }
  }
  __syncthreads();
  {
    char* dst = smem + wave * 10240 + lane * 16;
#pragma unroll
    for (int n = 0; n < 9; ++n)
#pragma unroll
      for (int m = 0; m < 2; ++m) {
        bool mine = (wk == 0) ? (n < 5) : (n >= 5);
        int slot = (wk == 0) ? (n - 5) : n;
        if (!mine) *(f32x4*)(dst + (slot * 2 + m) * 1024) = acc[n][m];
      }
  }
  __syncthreads();
  {
    const char* s = smem + (wave ^ 4) * 10240 + lane * 16;
#pragma unroll
    for (int n = 0; n < 9; ++n)
#pragma unroll
      for (int m = 0; m < 2; ++m) {
        bool mine = (wk == 0) ? (n < 5) : (n >= 5);
        int slot = (wk == 0) ? n : (n - 5);
        if (mine) {
          f32x4 v = acc[n][m] + *(const f32x4*)(s + (slot * 2 + m) * 1024);
          size_t off = (size_t)(t0 + wt * 32 + m * 16 + fr) * D_ + e0 + n * 16 + fq * 4;
          if (sp < 2) {
            u16x4 w;
#pragma unroll
            for (int r = 0; r < 4; ++r) w[r] = f2bf(v[r]);
            *(u16x4*)((sp == 0 ? yre : ynim) + off) = w;
          } else {
            *(f32x4*)((sp == 2 ? olo : ohi) + off) = v;
          }
        }
      }
  }
}

__global__ __launch_bounds__(256) void k3_combine(const u16* __restrict__ x, const u16* __restrict__ yre,
                                                  const u16* __restrict__ ynim, const float* __restrict__ olo,
                                                  const float* __restrict__ ohi, u16* __restrict__ out) {
  const size_t i = ((size_t)blockIdx.x * 256 + threadIdx.x) * 8;
  u16x8 xv = *(const u16x8*)(x + i), a = *(const u16x8*)(yre + i), b = *(const u16x8*)(ynim + i);
  f32x4 l0 = *(const f32x4*)(olo + i), l1 = *(const f32x4*)(olo + i + 4);
  f32x4 h0 = *(const f32x4*)(ohi + i), h1 = *(const f32x4*)(ohi + i + 4);
  u16x8 r;
#pragma unroll
  for (int j = 0; j < 8; ++j) {
    float s = rbf(bf2f(a[j]) + bf2f(b[j]));
    float o = rbf((j < 4 ? l0[j] : l1[j - 4]) + (j < 4 ? h0[j] : h1[j - 4]));
    float g = rbf(o * sigmoidf_(o));
    r[j] = f2bf(bf2f(xv[j]) + rbf(s * g));
  }
  *(u16x8*)(out + i) = r;
}

extern "C" void kernel_launch(void* const* d_in, const int* in_sizes, int n_in,
                              void* d_out, int out_size, void* d_ws, size_t ws_size,
                              hipStream_t stream) {
  const u16* x = (const u16*)d_in[0];
  const u16* sre = (const u16*)d_in[1];
  const u16* sim = (const u16*)d_in[2];
  const u16* wre = (const u16*)d_in[3];
  const u16* wim = (const u16*)d_in[4];
  const u16* wg = (const u16*)d_in[6];
  const u16* wu = (const u16*)d_in[7];
  const u16* wd = (const u16*)d_in[8];
  char* ws = (char*)d_ws;
  const size_t SD2 = (size_t)S_ * D_ * 2;
  u16* xn = (u16*)(ws);
  u16* a = (u16*)(ws + SD2);
  u16* nb = (u16*)(ws + 2 * SD2);
  u16* yre = (u16*)(ws + 3 * SD2);
  u16* ynim = (u16*)(ws + 4 * SD2);
  float* olo = (float*)(ws + 5 * SD2);
  float* ohi = (float*)(ws + 7 * SD2);
  u16* h = (u16*)(ws + 9 * SD2);
  (void)hipFuncSetAttribute((const void*)k1_glu, hipFuncAttributeMaxDynamicSharedMemorySize, K1_LDS);
  (void)hipFuncSetAttribute((const void*)k2_gemm, hipFuncAttributeMaxDynamicSharedMemorySize, K2_LDS);
  k0_ln<<<S_ / 16, 256, 0, stream>>>(x, xn);
  k1_glu<<<256, 768, K1_LDS, stream>>>(xn, wg, wu, wre, wim, h, a, nb);
  k2_gemm<<<256, 512, K2_LDS, stream>>>(sre, sim, a, nb, h, wd, yre, ynim, olo, ohi);
  k3_combine<<<(S_ * D_ / 8) / 256, 256, 0, stream>>>(x, yre, ynim, olo, ohi, (u16*)d_out);
}
```

```cpp
#include <hip/hip_runtime.h>
#include <hip/hip_bf16.h>

typedef unsigned short u16;
typedef __attribute__((ext_vector_type(8))) short bf16x8;
typedef __attribute__((ext_vector_type(4))) short bf16x4;
typedef __attribute__((ext_vector_type(4))) float f32x4;
typedef __attribute__((ext_vector_type(4))) unsigned short u16x4;
typedef __attribute__((ext_vector_type(8))) unsigned short u16x8;
typedef __attribute__((address_space(3))) bf16x4 lds_bf16x4;

constexpr int S_ = 2048, D_ = 576, F_ = 4608;

__device__ __forceinline__ float bf2f(u16 v) { return __uint_as_float(((unsigned)v) << 16); }
__device__ __forceinline__ u16 f2bf(float f) { __bf16 b = (__bf16)f; return __builtin_bit_cast(u16, b); }
__device__ __forceinline__ float rbf(float f) { return bf2f(f2bf(f)); }
__device__ __forceinline__ float sigmoidf_(float x) { return 1.0f / (1.0f + __expf(-x)); }

__device__ __forceinline__ void glds16s(const void* sbase, unsigned voff, unsigned lds_dst) {
  unsigned keep;
  asm volatile("s_mov_b32 %0, m0\n\ts_mov_b32 m0, %3\n\ts_nop 0\n\tglobal_load_lds_dwordx4 %1, %2\n\ts_mov_b32 m0, %0"
               : "=&s"(keep) : "v"(voff), "s"(sbase), "s"(lds_dst) : "memory");
}
__device__ __forceinline__ bf16x4 tr_read(const char* p) {
  return __builtin_amdgcn_ds_read_tr16_b64_v4i16((lds_bf16x4*)p);
}
__device__ __forceinline__ bf16x8 cat2(bf16x4 lo, bf16x4 hi) {
  return __builtin_shufflevector(lo, hi, 0, 1, 2, 3, 4, 5, 6, 7);
}
#define MFMA16(a, b, c) __builtin_amdgcn_mfma_f32_16x16x32_bf16((a), (b), (c), 0, 0, 0)
#define WAIT_V(n) asm volatile("s_waitcnt vmcnt(" #n ")" ::: "memory")
__device__ __forceinline__ int swz64(int row) { return (-(row >> 2)) & 3; }

__global__ __launch_bounds__(256) void k0_ln(const u16* __restrict__ x, u16* __restrict__ xn) {
  const int tid = threadIdx.x;
  const int row = blockIdx.x * 16 + (tid >> 4), l = tid & 15;
  const u16* xr = x + (size_t)row * D_ + l * 4;
  u16x4 v[9];
#pragma unroll
  for (int j = 0; j < 9; ++j) v[j] = *(const u16x4*)(xr + j * 64);
  float sum = 0.f;
#pragma unroll
  for (int j = 0; j < 9; ++j)
#pragma unroll
    for (int e = 0; e < 4; ++e) sum += bf2f(v[j][e]);
  sum += __shfl_xor(sum, 1); sum += __shfl_xor(sum, 2); sum += __shfl_xor(sum, 4); sum += __shfl_xor(sum, 8);
  const float mean = sum * (1.0f / D_);
  float vs = 0.f;
#pragma unroll
  for (int j = 0; j < 9; ++j)
#pragma unroll
    for (int e = 0; e < 4; ++e) { float d = bf2f(v[j][e]) - mean; vs += d * d; }
  vs += __shfl_xor(vs, 1); vs += __shfl_xor(vs, 2); vs += __shfl_xor(vs, 4); vs += __shfl_xor(vs, 8);
  const float mb = rbf(mean);
  const float vb = rbf(vs * (1.0f / D_));
  const float den = rbf(sqrtf(rbf(vb + 1e-5f)));
  const int r = row & 15;
  char* xo = (char*)xn + (size_t)(row >> 4) * 18 * 1024 + r * 64;
#pragma unroll
  for (int j = 0; j < 9; ++j) {
    u16x4 t;
#pragma unroll
    for (int e = 0; e < 4; ++e) t[e] = f2bf(rbf(bf2f(v[j][e]) - mb) / den);
    int col = j * 64 + l * 4;
    int kb = col >> 5, c = (col & 31) >> 3;
    *(u16x4*)(xo + kb * 1024 + ((c ^ swz64(r)) * 16) + (col & 7) * 2) = t;
  }
}

constexpr int K1_XS = 256 * 32 * 2;
constexpr int K1_GS = 32 * 144 * 2;
constexpr int K1_DS = 48 * 32 * 2;
constexpr int K1_SLOT = K1_XS + 2 * K1_GS + K1_DS;
constexpr int K1_LDS = 4 * K1_SLOT;
__global__ __launch_bounds__(768) void k1_glu(const u16* __restrict__ xn, const u16* __restrict__ wg,
                                              const u16* __restrict__ wu, const u16* __restrict__ wre,
                                              const u16* __restrict__ wim, u16* __restrict__ h,
                                              u16* __restrict__ a, u16* __restrict__ nb) {
  extern __shared__ __attribute__((aligned(16))) char smem[];
  const int tid = threadIdx.x, lane = tid & 63, wave = __builtin_amdgcn_readfirstlane(tid >> 6);
  const int ws = wave & 3, wf = wave >> 2;
  const int xcd = blockIdx.x & 7, idx = blockIdx.x >> 3;
  const int ft = xcd * 4 + (idx >> 3), st = idx & 7;
  const int f0 = ft * 144, s0 = st * 256;
  const bool hasab = ft < 24;
  const int np = hasab ? 37 : 34;
  const u16* wdf = (ft < 12 ? wre + (size_t)ft * 48 * D_ : wim + (size_t)(hasab ? ft - 12 : 0) * 48 * D_);
  const int fr = lane & 15, fq = lane >> 4;
  unsigned voff[4];
#pragma unroll
  for (int j = 0; j < 4; ++j) {
    int i = wave + 12 * j;
    if (i < 16) {
      voff[j] = (unsigned)((s0 / 16 + i) * 18 * 1024 + lane * 16);
    } else if (i >= 34) {
      int r = ((i < 37) ? i - 34 : 0) * 16 + (lane >> 2);
      int c = (lane & 3) ^ swz64(r);
      voff[j] = (unsigned)(r * D_ + c * 8) * 2u;
    } else {
      int jj = (i - 16) % 9;
      int sb = jj * 4 + (lane >> 4), ii = lane & 15;
      int kb = sb / 9, nbk = sb % 9;
      int r = (ii >> 1) ^ ((kb & 1) << 2);
      voff[j] = (unsigned)((kb * 8 + r) * F_ + f0 + nbk * 16 + (ii & 1) * 8) * 2u;
    }
  }
  const int P = (wave + 36 < np) ? 4 : ((wave + 24 < np) ? 3 : 2);
  const unsigned lds0 = __builtin_amdgcn_readfirstlane((unsigned)(size_t)smem);
  auto piece = [&](int kt, int j) {
    const unsigned dst = lds0 + (kt & 3) * K1_SLOT;
    int i = wave + 12 * j;
    const u16* base = (i < 16) ? (xn + kt * 512)
                    : (i < 34) ? ((i < 25 ? wg : wu) + (size_t)kt * 32 * F_) : (wdf + kt * 32);
    if (i < np) glds16s(base, voff[j], dst + i * 1024);
  };
  auto stage = [&](int kt) {
#pragma unroll
    for (int j = 0; j < 4; ++j) piece(kt, j);
  };
  f32x4 accg[3][4] = {}, accu[3][4] = {}, accd[4] = {};
  const int q = (lane & 15) >> 2, p = lane & 3;
  const int sw = (fq & 1) << 2;
  stage(0); stage(1); stage(2);
#pragma unroll 1
  for (int kt = 0; kt < 18; ++kt) {
    const int rem = 17 - kt;
    const int n = P * (rem < 2 ? rem : 2);
    if (n >= 8) WAIT_V(8); else if (n >= 6) WAIT_V(6); else if (n >= 4) WAIT_V(4);
    else if (n >= 3) WAIT_V(3); else if (n >= 2) WAIT_V(2); else WAIT_V(0);
    __syncthreads();
    const bool pf = kt + 3 < 18;
    const char* xb = smem + (kt & 3) * K1_SLOT;
    const char* wb = xb + K1_XS;
    const char* db = wb + 2 * K1_GS;
    bf16x8 xf[4], gf[3], uf[3], df;
#pragma unroll
    for (int m = 0; m < 4; ++m) {
      int r = ws * 64 + m * 16 + fr;
      xf[m] = *(const bf16x8*)(xb + r * 64 + ((fq ^ swz64(r)) * 16));
    }
#pragma unroll
    for (int t = 0; t < 3; ++t) {
      const char* bg = wb + (fq * 9 + wf * 3 + t) * 256 + p * 8;
      const char* bu = bg + K1_GS;
      gf[t] = cat2(tr_read(bg + ((q ^ sw) * 32)), tr_read(bg + (((q + 4) ^ sw) * 32)));
      uf[t] = cat2(tr_read(bu + ((q ^ sw) * 32)), tr_read(bu + (((q + 4) ^ sw) * 32)));
    }
    {
      int r = wf * 16 + fr;
      df = *(const bf16x8*)(db + r * 64 + ((fq ^ swz64(r)) * 16));
    }
#pragma unroll
    for (int t = 0; t < 3; ++t) {
#pragma unroll
      for (int m = 0; m < 4; ++m) {
        accg[t][m] = MFMA16(gf[t], xf[m], accg[t][m]);
        accu[t][m] = MFMA16(uf[t], xf[m], accu[t][m]);
      }
      __builtin_amdgcn_sched_barrier(0);
      if (pf) piece(kt + 3, t);
      __builtin_amdgcn_sched_barrier(0);
    }
    if (hasab) {
#pragma unroll
      for (int m = 0; m < 4; ++m) accd[m] = MFMA16(df, xf[m], accd[m]);
    }
    __builtin_amdgcn_sched_barrier(0);
    if (pf) piece(kt + 3, 3);
  }
#pragma unroll
  for (int t = 0; t < 3; ++t)
#pragma unroll
    for (int m = 0; m < 4; ++m) {
      u16x4 v;
#pragma unroll
      for (int r = 0; r < 4; ++r) {
        float g = rbf(accg[t][m][r]), u = rbf(accu[t][m][r]);
        v[r] = f2bf(rbf(sigmoidf_(g)) * u);
      }
      int srow = s0 + ws * 64 + m * 16 + fr;
      *(u16x4*)(h + (size_t)srow * F_ + f0 + wf * 48 + t * 16 + fq * 4) = v;
    }
  if (hasab) {
    const bool isim = ft >= 12;
    const int e = (isim ? ft - 12 : ft) * 48 + wf * 16;
    const int nt = e / 144, nbk = (e % 144) >> 4;
    char* o = (char*)(isim ? nb : a) + (size_t)nt * 32 * 18432 + nbk * 256 + fq * 8;
    const float sg = isim ? -1.f : 1.f;
#pragma unroll
    for (int m = 0; m < 4; ++m) {
      u16x4 v;
#pragma unroll
      for (int r = 0; r < 4; ++r) v[r] = f2bf(sg * accd[m][r]);
      int s = s0 + ws * 64 + m * 16 + fr;
      int stg = s >> 6, kb = (s & 63) >> 3, rr = (s & 7) ^ ((kb & 1) << 2);
      *(u16x4*)(o + (size_t)stg * 18432 + kb * 2304 + rr * 32) = v;
    }
  }
}

constexpr int K2_AS = 128 * 64 * 2;
constexpr int K2_BS = 64 * 144 * 2;
constexpr int K2_SLOT = K2_AS + K2_BS;
constexpr int K2_LDS = 4 * K2_SLOT;
__global__ __launch_bounds__(512) void k2_gemm(const u16* __restrict__ sre, const u16* __restrict__ sim,
                                               const u16* __restrict__ a, const u16* __restrict__ nb,
                                               const u16* __restrict__ h, const u16* __restrict__ wd,
                                               u16* __restrict__ yre, u16* __restrict__ ynim,
                                               float* __restrict__ olo, float* __restrict__ ohi) {
  extern __shared__ __attribute__((aligned(16))) char smem[];
  const int tid = threadIdx.x, lane = tid & 63, wave = __builtin_amdgcn_readfirstlane(tid >> 6);
  const int xcd = blockIdx.x & 7, idx = blockIdx.x >> 3;
  const int sp = xcd >> 1;
  const int mt = (xcd & 1) * 8 + (idx >> 2), nt = idx & 3;
  const int t0 = mt * 128, e0 = nt * 144;
  const int nst = sp < 2 ? 32 : 36;
  if (wave >= 4) {
    const int lw = wave - 4;
    const u16* A; const u16* B; int lda; int bstep;
    const bool img = sp < 2;
    if (sp == 0) { A = sre; B = a + (size_t)nt * 32 * 9216; lda = S_; bstep = 9216; }
    else if (sp == 1) { A = sim; B = nb + (size_t)nt * 32 * 9216; lda = S_; bstep = 9216; }
    else if (sp == 2) { A = h; B = wd; lda = F_; bstep = 64 * D_; }
    else { A = h + 2304; B = wd + (size_t)2304 * D_; lda = F_; bstep = 64 * D_; }
    unsigned voff[9];
#pragma unroll
    for (int j = 0; j < 9; ++j) {
      int i = lw + 4 * j;
      if (i < 16) {
        int r = i * 8 + (lane >> 3);
        int c = (lane & 7) ^ (lane >> 3);
        voff[j] = (unsigned)((t0 + r) * lda + c * 8) * 2u;
      } else {
        int jj = (i < 34) ? (i - 16) : 0;
        if (img) {
          voff[j] = (unsigned)(jj * 1024 + lane * 16);
        } else {
          int sb = jj * 4 + (lane >> 4), ii = lane & 15;
          int kb = sb / 9, nbk = sb % 9;
          int r = (ii >> 1) ^ ((kb & 1) << 2);
          voff[j] = (unsigned)((kb * 8 + r) * D_ + e0 + nbk * 16 + (ii & 1) * 8) * 2u;
        }
      }
    }
    const bool p9 = lw < 2;
    const unsigned lds0 = __builtin_amdgcn_readfirstlane((unsigned)(size_t)smem);
    auto stage = [&](int kt) {
#pragma unroll
      for (int j = 0; j < 9; ++j) {
        const u16* base = (j < 4) ? (A + kt * 64) : (B + (size_t)kt * bstep);
        if (j < 8 || p9) glds16s(base, voff[j], lds0 + (kt & 3) * K2_SLOT + (lw + 4 * j) * 1024);
      }
    };
    stage(0); stage(1); stage(2); stage(3);
    if (p9) WAIT_V(27); else WAIT_V(24);
    __syncthreads();
#pragma unroll 1
    for (int kt = 0; kt < nst; ++kt) {
      const int rem = nst - 2 - kt;
      if (rem >= 2) { if (p9) WAIT_V(18); else WAIT_V(16); }
      else if (rem == 1) { if (p9) WAIT_V(9); else WAIT_V(8); }
      else WAIT_V(0);
      __syncthreads();
      if (kt + 4 < nst) stage(kt + 4);
    }
    __syncthreads();
    __syncthreads();
    return;
  }
  const int wt = wave & 1, wk = wave >> 1;
  const int fr = lane & 15, fq = lane >> 4;
  const int q = (lane & 15) >> 2, p = lane & 3;
  const int sw = (fq & 1) << 2;
  f32x4 acc[9][4] = {};
  bf16x8 afA[4], afB[4];
  bf16x4 blA[9], bhA[9], blB[9], bhB[9];
  const int aoff = (wt * 64 + fr) * 128 + (((wk * 4 + fq) ^ (fr & 7)) * 16);
  const int boff0 = K2_AS + (wk * 4 + fq) * 9 * 256 + p * 8 + ((q ^ sw) * 32);
  const int boff1 = K2_AS + (wk * 4 + fq) * 9 * 256 + p * 8 + (((q + 4) ^ sw) * 32);
#define RD(AFN, BLN, BHN, r, base)                                              \
  do {                                                                          \
    if ((r) < 4) AFN[(r) & 3] = *(const bf16x8*)((base) + aoff + ((r) & 3) * 2048); \
    else if ((((r) - 4) & 1) == 0) BLN[((r) - 4) >> 1] = tr_read((base) + boff0 + (((r) - 4) >> 1) * 256); \
    else BHN[((r) - 4) >> 1] = tr_read((base) + boff1 + (((r) - 4) >> 1) * 256); \
  } while (0)
#define ITER(AFC, BLC, BHC, AFN, BLN, BHN, KT)                                  \
  do {                                                                          \
    __syncthreads();                                                            \
    const char* nb_ = smem + (((KT) + 1) & 3) * K2_SLOT;                        \
    _Pragma("unroll") for (int i = 0; i < 36; ++i) {                            \
      acc[i >> 2][i & 3] = MFMA16(cat2(BLC[i >> 2], BHC[i >> 2]), AFC[i & 3], acc[i >> 2][i & 3]); \
      __builtin_amdgcn_sched_barrier(0);                                        \
      if (i < 22) { RD(AFN, BLN, BHN, i, nb_); __builtin_amdgcn_sched_barrier(0); } \
    }                                                                           \
  } while (0)
  __syncthreads();
#pragma unroll
  for (int r = 0; r < 22; ++r) RD(afA, blA, bhA, r, smem);
#pragma unroll 1
  for (int kt = 0; kt < nst; kt += 2) {
    ITER(afA, blA, bhA, afB, blB, bhB, kt);
    ITER(afB, blB, bhB, afA, blA, bhA, kt + 1);
  }
  __syncthreads();
  {
    char* dst = smem + wave * 20480 + lane * 16;
#pragma unroll
    for (int n = 0; n < 9; ++n)
#pragma unroll
      for (int m = 0; m < 4; ++m) {
        bool mine = (wk == 0) ? (n < 5) : (n >= 5);
        int slot = (wk == 0) ? (n - 5) : n;
        if (!mine) *(f32x4*)(dst + (slot * 4 + m) * 1024) = acc[n][m];
      }
  }
  __syncthreads();
  {
    const char* s = smem + (wave ^ 2) * 20480 + lane * 16;
#pragma unroll
    for (int n = 0; n < 9; ++n)
#pragma unroll
      for (int m = 0; m < 4; ++m) {
        bool mine = (wk == 0) ? (n < 5) : (n >= 5);
        int slot = (wk == 0) ? n : (n - 5);
        if (mine) {
          f32x4 v = acc[n][m] + *(const f32x4*)(s + (slot * 4 + m) * 1024);
          size_t off = (size_t)(t0 + wt * 64 + m * 16 + fr) * D_ + e0 + n * 16 + fq * 4;
          if (sp < 2) {
            u16x4 w;
#pragma unroll
            for (int r = 0; r < 4; ++r) w[r] = f2bf(v[r]);
            *(u16x4*)((sp == 0 ? yre : ynim) + off) = w;
          } else {
            *(f32x4*)((sp == 2 ? olo : ohi) + off) = v;
          }
        }
      }
  }
}

__global__ __launch_bounds__(256) void k3_combine(const u16* __restrict__ x, const u16* __restrict__ yre,
                                                  const u16* __restrict__ ynim, const float* __restrict__ olo,
                                                  const float* __restrict__ ohi, u16* __restrict__ out) {
  const size_t i = ((size_t)blockIdx.x * 256 + threadIdx.x) * 8;
  u16x8 xv = *(const u16x8*)(x + i), a = *(const u16x8*)(yre + i), b = *(const u16x8*)(ynim + i);
  f32x4 l0 = *(const f32x4*)(olo + i), l1 = *(const f32x4*)(olo + i + 4);
  f32x4 h0 = *(const f32x4*)(ohi + i), h1 = *(const f32x4*)(ohi + i + 4);
  u16x8 r;
#pragma unroll
  for (int j = 0; j < 8; ++j) {
    float s = rbf(bf2f(a[j]) + bf2f(b[j]));
    float o = rbf((j < 4 ? l0[j] : l1[j - 4]) + (j < 4 ? h0[j] : h1[j - 4]));
    float g = rbf(o * sigmoidf_(o));
    r[j] = f2bf(bf2f(xv[j]) + rbf(s * g));
  }
  *(u16x8*)(out + i) = r;
}

extern "C" void kernel_launch(void* const* d_in, const int* in_sizes, int n_in,
                              void* d_out, int out_size, void* d_ws, size_t ws_size,
                              hipStream_t stream) {
  const u16* x = (const u16*)d_in[0];
  const u16* sre = (const u16*)d_in[1];
  const u16* sim = (const u16*)d_in[2];
  const u16* wre = (const u16*)d_in[3];
  const u16* wim = (const u16*)d_in[4];
  const u16* wg = (const u16*)d_in[6];
  const u16* wu = (const u16*)d_in[7];
  const u16* wd = (const u16*)d_in[8];
  char* ws = (char*)d_ws;
  const size_t SD2 = (size_t)S_ * D_ * 2;
  u16* xn = (u16*)(ws);
  u16* a = (u16*)(ws + SD2);
  u16* nb = (u16*)(ws + 2 * SD2);
  u16* yre = (u16*)(ws + 3 * SD2);
  u16* ynim = (u16*)(ws + 4 * SD2);
  float* olo = (float*)(ws + 5 * SD2);
  float* ohi = (float*)(ws + 7 * SD2);
  u16* h = (u16*)(ws + 9 * SD2);
  (void)hipFuncSetAttribute((const void*)k1_glu, hipFuncAttributeMaxDynamicSharedMemorySize, K1_LDS);
  (void)hipFuncSetAttribute((const void*)k2_gemm, hipFuncAttributeMaxDynamicSharedMemorySize, K2_LDS);
  k0_ln<<<S_ / 16, 256, 0, stream>>>(x, xn);
  k1_glu<<<256, 768, K1_LDS, stream>>>(xn, wg, wu, wre, wim, h, a, nb);
  k2_gemm<<<256, 512, K2_LDS, stream>>>(sre, sim, a, nb, h, wd, yre, ynim, olo, ohi);
  k3_combine<<<(S_ * D_ / 8) / 256, 256, 0, stream>>>(x, yre, ynim, olo, ohi, (u16*)d_out);
}
```

```cpp
#include <hip/hip_runtime.h>
#include <hip/hip_bf16.h>

typedef unsigned short u16;
typedef __attribute__((ext_vector_type(8))) short bf16x8;
typedef __attribute__((ext_vector_type(4))) short bf16x4;
typedef __attribute__((ext_vector_type(4))) float f32x4;
typedef __attribute__((ext_vector_type(4))) unsigned short u16x4;
typedef __attribute__((ext_vector_type(8))) unsigned short u16x8;
typedef __attribute__((address_space(3))) bf16x4 lds_bf16x4;

constexpr int S_ = 2048, D_ = 576, F_ = 4608;

__device__ __forceinline__ float bf2f(u16 v) { return __uint_as_float(((unsigned)v) << 16); }
__device__ __forceinline__ u16 f2bf(float f) { __bf16 b = (__bf16)f; return __builtin_bit_cast(u16, b); }
__device__ __forceinline__ float rbf(float f) { return bf2f(f2bf(f)); }
__device__ __forceinline__ float sigmoidf_(float x) { return 1.0f / (1.0f + __expf(-x)); }

__device__ __forceinline__ void glds16s(const void* sbase, unsigned voff, unsigned lds_dst) {
  unsigned keep;
  asm volatile("s_mov_b32 %0, m0\n\ts_mov_b32 m0, %3\n\ts_nop 0\n\tglobal_load_lds_dwordx4 %1, %2\n\ts_mov_b32 m0, %0"
               : "=&s"(keep) : "v"(voff), "s"(sbase), "s"(lds_dst) : "memory");
}
__device__ __forceinline__ bf16x4 tr_read(const char* p) {
  return __builtin_amdgcn_ds_read_tr16_b64_v4i16((lds_bf16x4*)p);
}
__device__ __forceinline__ bf16x8 cat2(bf16x4 lo, bf16x4 hi) {
  return __builtin_shufflevector(lo, hi, 0, 1, 2, 3, 4, 5, 6, 7);
}
#define MFMA16(a, b, c) __builtin_amdgcn_mfma_f32_16x16x32_bf16((a), (b), (c), 0, 0, 0)
#define WAIT_V(n) asm volatile("s_waitcnt vmcnt(" #n ")" ::: "memory")
__device__ __forceinline__ int swz64(int row) { return (-(row >> 2)) & 3; }

__global__ __launch_bounds__(256) void k0_ln(const u16* __restrict__ x, u16* __restrict__ xn) {
  const int tid = threadIdx.x;
  const int row = blockIdx.x * 16 + (tid >> 4), l = tid & 15;
  const u16* xr = x + (size_t)row * D_ + l * 4;
  u16x4 v[9];
#pragma unroll
  for (int j = 0; j < 9; ++j) v[j] = *(const u16x4*)(xr + j * 64);
  float sum = 0.f;
#pragma unroll
  for (int j = 0; j < 9; ++j)
#pragma unroll
    for (int e = 0; e < 4; ++e) sum += bf2f(v[j][e]);
  sum += __shfl_xor(sum, 1); sum += __shfl_xor(sum, 2); sum += __shfl_xor(sum, 4); sum += __shfl_xor(sum, 8);
  const float mean = sum * (1.0f / D_);
  float vs = 0.f;
#pragma unroll
  for (int j = 0; j < 9; ++j)
#pragma unroll
    for (int e = 0; e < 4; ++e) { float d = bf2f(v[j][e]) - mean; vs += d * d; }
  vs += __shfl_xor(vs, 1); vs += __shfl_xor(vs, 2); vs += __shfl_xor(vs, 4); vs += __shfl_xor(vs, 8);
  const float mb = rbf(mean);
  const float vb = rbf(vs * (1.0f / D_));
  const float den = rbf(sqrtf(rbf(vb + 1e-5f)));
  const int r = row & 15;
  char* xo = (char*)xn + (size_t)(row >> 4) * 18 * 1024 + r * 64;
#pragma unroll
  for (int j = 0; j < 9; ++j) {
    u16x4 t;
#pragma unroll
    for (int e = 0; e < 4; ++e) t[e] = f2bf(rbf(bf2f(v[j][e]) - mb) / den);
    int col = j * 64 + l * 4;
    int kb = col >> 5, c = (col & 31) >> 3;
    *(u16x4*)(xo + kb * 1024 + ((c ^ swz64(r)) * 16) + (col & 7) * 2) = t;
  }
}

constexpr int K1_XS = 256 * 32 * 2;
constexpr int K1_GS = 32 * 144 * 2;
constexpr int K1_DS = 48 * 32 * 2;
constexpr int K1_SLOT = K1_XS + 2 * K1_GS + K1_DS;
constexpr int K1_LDS = 4 * K1_SLOT;

template <int NP, int NE, int PB, int EB>
__device__ __forceinline__ void k1_wave(char* smem, const u16* __restrict__ xn, const u16* __restrict__ wg,
                                        const u16* __restrict__ wu, const u16* __restrict__ wdf,
                                        u16* __restrict__ h, u16* __restrict__ a, u16* __restrict__ nb,
                                        int wave, int lane, int ft, int st) {
  const int ws = wave & 3;
  const int f0 = ft * 144, s0 = st * 256;
  const bool hasab = ft < 24;
  const int np = hasab ? 37 : 34;
  const int fr = lane & 15, fq = lane >> 4;
  unsigned voff[5];
#pragma unroll
  for (int j = 0; j < 5; ++j) {
    int i = wave + 8 * j;
    if (i < 16) {
      voff[j] = (unsigned)((s0 / 16 + i) * 18 * 1024 + lane * 16);
    } else if (i >= 34) {
      int r = ((i < 37) ? i - 34 : 0) * 16 + (lane >> 2);
      int c = (lane & 3) ^ swz64(r);
      voff[j] = (unsigned)(r * D_ + c * 8) * 2u;
    } else {
      int jj = (i - 16) % 9;
      int sb = jj * 4 + (lane >> 4), ii = lane & 15;
      int kb = sb / 9, nbk = sb % 9;
      int r = (ii >> 1) ^ ((kb & 1) << 2);
      voff[j] = (unsigned)((kb * 8 + r) * F_ + f0 + nbk * 16 + (ii & 1) * 8) * 2u;
    }
  }
  const bool p5 = wave + 32 < np;
  const unsigned lds0 = __builtin_amdgcn_readfirstlane((unsigned)(size_t)smem);
  auto piece = [&](int kt, int j) {
    int i = wave + 8 * j;
    const u16* base = (i < 16) ? (xn + kt * 512)
                    : (i < 34) ? ((i < 25 ? wg : wu) + (size_t)kt * 32 * F_) : (wdf + kt * 32);
    if (j < 4 || p5) glds16s(base, voff[j], lds0 + (kt & 3) * K1_SLOT + i * 1024);
  };
  f32x4 accg[NP][4] = {}, accu[NP][4] = {}, accd[NE][4] = {};
  bf16x8 xf[2][4], df[NE];
  bf16x4 gl[2], gh[2], ul[2], uh[2];
  const int q = (lane & 15) >> 2, p = lane & 3;
  const int sw = (fq & 1) << 2;
  const int xoff = (ws * 64 + fr) * 64 + ((fq ^ swz64(fr)) * 16);
  const int goff0 = K1_XS + (fq * 9 + PB) * 256 + p * 8 + ((q ^ sw) * 32);
  const int goff1 = K1_XS + (fq * 9 + PB) * 256 + p * 8 + (((q + 4) ^ sw) * 32);
  const int doff = K1_XS + 2 * K1_GS + (EB * 16 + fr) * 64 + ((fq ^ swz64(fr)) * 16);
#define K1_RDGU(bi, sb, t, r)                                                   \
  do {                                                                          \
    if ((r) == 0) gl[bi] = tr_read((sb) + goff0 + (t) * 256);                   \
    else if ((r) == 1) gh[bi] = tr_read((sb) + goff1 + (t) * 256);              \
    else if ((r) == 2) ul[bi] = tr_read((sb) + goff0 + (t) * 256 + K1_GS);      \
    else uh[bi] = tr_read((sb) + goff1 + (t) * 256 + K1_GS);                    \
  } while (0)
#define K1_FENCE __builtin_amdgcn_sched_barrier(0)
#define K1_STAGE(SP, KT)                                                        \
  do {                                                                          \
    if ((KT) + 2 < 18) { if (p5) WAIT_V(5); else WAIT_V(4); } else WAIT_V(0);   \
    __syncthreads();                                                            \
    const char* cb = smem + ((KT) & 3) * K1_SLOT;                               \
    const char* nx = smem + (((KT) + 1) & 3) * K1_SLOT;                         \
    const bool pf = (KT) + 3 < 18;                                              \
    _Pragma("unroll") for (int t = 0; t < NP; ++t) {                            \
      constexpr int dummy_ = 0; (void)dummy_;                                   \
      const int bc = ((SP) * NP + t) & 1, bn = bc ^ 1;                          \
      _Pragma("unroll") for (int i = 0; i < 8; ++i) {                           \
        const int m = i >> 1;                                                   \
        if ((i & 1) == 0) accg[t][m] = MFMA16(cat2(gl[bc], gh[bc]), xf[SP][m], accg[t][m]); \
        else accu[t][m] = MFMA16(cat2(ul[bc], uh[bc]), xf[SP][m], accu[t][m]);  \
        K1_FENCE;                                                               \
        if (i < 4) {                                                            \
          if (t + 1 < NP) K1_RDGU(bn, cb, t + 1, i); else K1_RDGU(bn, nx, 0, i); \
        } else if (t == NP - 1) {                                               \
          xf[(SP) ^ 1][i - 4] = *(const bf16x8*)(nx + xoff + (i - 4) * 1024);   \
        } else if (t == NP - 2 && i - 4 < NE) {                                 \
          df[i - 4] = *(const bf16x8*)(cb + doff + (i - 4) * 1024);             \
        } else if (i >= 6 && 2 * t + (i - 6) < 5) {                             \
          if (pf) piece((KT) + 3, 2 * t + (i - 6));                             \
        }                                                                       \
        K1_FENCE;                                                               \
      }                                                                         \
    }                                                                           \
    if (hasab) {                                                                \
      _Pragma("unroll") for (int e = 0; e < NE; ++e)                            \
        _Pragma("unroll") for (int m = 0; m < 4; ++m) accd[e][m] = MFMA16(df[e], xf[SP][m], accd[e][m]); \
    }                                                                           \
  } while (0)
#pragma unroll
  for (int s = 0; s < 3; ++s)
#pragma unroll
    for (int j = 0; j < 5; ++j) piece(s, j);
  if (p5) WAIT_V(10); else WAIT_V(8);
  __syncthreads();
#pragma unroll
  for (int m = 0; m < 4; ++m) xf[0][m] = *(const bf16x8*)(smem + xoff + m * 1024);
#pragma unroll
  for (int r = 0; r < 4; ++r) K1_RDGU(0, smem, 0, r);
#pragma unroll 1
  for (int kt = 0; kt < 18; kt += 2) {
    K1_STAGE(0, kt);
    K1_STAGE(1, kt + 1);
  }
#pragma unroll
  for (int t = 0; t < NP; ++t)
#pragma unroll
    for (int m = 0; m < 4; ++m) {
      u16x4 v;
#pragma unroll
      for (int r = 0; r < 4; ++r) {
        float g = rbf(accg[t][m][r]), u = rbf(accu[t][m][r]);
        v[r] = f2bf(rbf(sigmoidf_(g)) * u);
      }
      int srow = s0 + ws * 64 + m * 16 + fr;
      *(u16x4*)(h + (size_t)srow * F_ + f0 + (PB + t) * 16 + fq * 4) = v;
    }
  if (hasab) {
    const bool isim = ft >= 12;
    const float sg = isim ? -1.f : 1.f;
#pragma unroll
    for (int e = 0; e < NE; ++e) {
      const int ec = (isim ? ft - 12 : ft) * 48 + (EB + e) * 16;
      const int nt = ec / 144, nbk = (ec % 144) >> 4;
      char* o = (char*)(isim ? nb : a) + (size_t)nt * 32 * 18432 + nbk * 256 + fq * 8;
#pragma unroll
      for (int m = 0; m < 4; ++m) {
        u16x4 v;
#pragma unroll
        for (int r = 0; r < 4; ++r) v[r] = f2bf(sg * accd[e][m][r]);
        int s = s0 + ws * 64 + m * 16 + fr;
        int stg = s >> 6, kb = (s & 63) >> 3, rr = (s & 7) ^ ((kb & 1) << 2);
        *(u16x4*)(o + (size_t)stg * 18432 + kb * 2304 + rr * 32) = v;
      }
    }
  }
}

__global__ __launch_bounds__(512) void k1_glu(const u16* __restrict__ xn, const u16* __restrict__ wg,
                                              const u16* __restrict__ wu, const u16* __restrict__ wre,
                                              const u16* __restrict__ wim, u16* __restrict__ h,
                                              u16* __restrict__ a, u16* __restrict__ nb) {
  extern __shared__ __attribute__((aligned(16))) char smem[];
  const int tid = threadIdx.x, lane = tid & 63, wave = __builtin_amdgcn_readfirstlane(tid >> 6);
  const int xcd = blockIdx.x & 7, idx = blockIdx.x >> 3;
  const int ft = xcd * 4 + (idx >> 3), st = idx & 7;
  const u16* wdf = (ft < 12 ? wre + (size_t)ft * 48 * D_ : wim + (size_t)(ft < 24 ? ft - 12 : 0) * 48 * D_);
  if (wave < 4) k1_wave<5, 1, 0, 0>(smem, xn, wg, wu, wdf, h, a, nb, wave, lane, ft, st);
  else k1_wave<4, 2, 5, 1>(smem, xn, wg, wu, wdf, h, a, nb, wave, lane, ft, st);
}

constexpr int K2_AS = 128 * 64 * 2;
constexpr int K2_BS = 64 * 144 * 2;
constexpr int K2_SLOT = K2_AS + K2_BS;
constexpr int K2_LDS = 4 * K2_SLOT;
__global__ __launch_bounds__(512) void k2_gemm(const u16* __restrict__ sre, const u16* __restrict__ sim,
                                               const u16* __restrict__ a, const u16* __restrict__ nb,
                                               const u16* __restrict__ h, const u16* __restrict__ wd,
                                               u16* __restrict__ yre, u16* __restrict__ ynim,
                                               float* __restrict__ olo, float* __restrict__ ohi) {
  extern __shared__ __attribute__((aligned(16))) char smem[];
  const int tid = threadIdx.x, lane = tid & 63, wave = __builtin_amdgcn_readfirstlane(tid >> 6);
  const int xcd = blockIdx.x & 7, idx = blockIdx.x >> 3;
  const int sp = xcd >> 1;
  const int mt = (xcd & 1) * 8 + (idx >> 2), nt = idx & 3;
  const int t0 = mt * 128, e0 = nt * 144;
  const int nst = sp < 2 ? 32 : 36;
  if (wave >= 4) {
    const int lw = wave - 4;
    const u16* A; const u16* B; int lda; int bstep;
    const bool img = sp < 2;
    if (sp == 0) { A = sre; B = a + (size_t)nt * 32 * 9216; lda = S_; bstep = 9216; }
    else if (sp == 1) { A = sim; B = nb + (size_t)nt * 32 * 9216; lda = S_; bstep = 9216; }
    else if (sp == 2) { A = h; B = wd; lda = F_; bstep = 64 * D_; }
    else { A = h + 2304; B = wd + (size_t)2304 * D_; lda = F_; bstep = 64 * D_; }
    unsigned voff[9];
#pragma unroll
    for (int j = 0; j < 9; ++j) {
      int i = lw + 4 * j;
      if (i < 16) {
        int r = i * 8 + (lane >> 3);
        int c = (lane & 7) ^ (lane >> 3);
        voff[j] = (unsigned)((t0 + r) * lda + c * 8) * 2u;
      } else {
        int jj = (i < 34) ? (i - 16) : 0;
        if (img) {
          voff[j] = (unsigned)(jj * 1024 + lane * 16);
        } else {
          int sb = jj * 4 + (lane >> 4), ii = lane & 15;
          int kb = sb / 9, nbk = sb % 9;
          int r = (ii >> 1) ^ ((kb & 1) << 2);
          voff[j] = (unsigned)((kb * 8 + r) * D_ + e0 + nbk * 16 + (ii & 1) * 8) * 2u;
        }
      }
    }
    const bool p9 = lw < 2;
    const unsigned lds0 = __builtin_amdgcn_readfirstlane((unsigned)(size_t)smem);
    auto stage = [&](int kt) {
#pragma unroll
      for (int j = 0; j < 9; ++j) {
        const u16* base = (j < 4) ? (A + kt * 64) : (B + (size_t)kt * bstep);
        if (j < 8 || p9) glds16s(base, voff[j], lds0 + (kt & 3) * K2_SLOT + (lw + 4 * j) * 1024);
      }
    };
    stage(0); stage(1); stage(2); stage(3);
    if (p9) WAIT_V(27); else WAIT_V(24);
    __syncthreads();
#pragma unroll 1
    for (int kt = 0; kt < nst; ++kt) {
      const int rem = nst - 2 - kt;
      if (rem >= 2) { if (p9) WAIT_V(18); else WAIT_V(16); }
      else if (rem == 1) { if (p9) WAIT_V(9); else WAIT_V(8); }
      else WAIT_V(0);
      __syncthreads();
      if (kt + 4 < nst) stage(kt + 4);
    }
    __syncthreads();
    __syncthreads();
    return;
  }
  const int wt = wave & 1, wk = wave >> 1;
  const int fr = lane & 15, fq = lane >> 4;
  const int q = (lane & 15) >> 2, p = lane & 3;
  const int sw = (fq & 1) << 2;
  f32x4 acc[9][4] = {};
  bf16x8 afA[4], afB[4];
  bf16x4 blA[9], bhA[9], blB[9], bhB[9];
  const int aoff = (wt * 64 + fr) * 128 + (((wk * 4 + fq) ^ (fr & 7)) * 16);
  const int boff0 = K2_AS + (wk * 4 + fq) * 9 * 256 + p * 8 + ((q ^ sw) * 32);
  const int boff1 = K2_AS + (wk * 4 + fq) * 9 * 256 + p * 8 + (((q + 4) ^ sw) * 32);
#define RD(AFN, BLN, BHN, r, base)                                              \
  do {                                                                          \
    if ((r) < 4) AFN[(r) & 3] = *(const bf16x8*)((base) + aoff + ((r) & 3) * 2048); \
    else if ((((r) - 4) & 1) == 0) BLN[((r) - 4) >> 1] = tr_read((base) + boff0 + (((r) - 4) >> 1) * 256); \
    else BHN[((r) - 4) >> 1] = tr_read((base) + boff1 + (((r) - 4) >> 1) * 256); \
  } while (0)
#define ITER(AFC, BLC, BHC, AFN, BLN, BHN, KT)                                  \
  do {                                                                          \
    __syncthreads();                                                            \
    const char* nb_ = smem + (((KT) + 1) & 3) * K2_SLOT;                        \
    _Pragma("unroll") for (int i = 0; i < 36; ++i) {                            \
      acc[i >> 2][i & 3] = MFMA16(cat2(BLC[i >> 2], BHC[i >> 2]), AFC[i & 3], acc[i >> 2][i & 3]); \
      __builtin_amdgcn_sched_barrier(0);                                        \
      if (i < 22) { RD(AFN, BLN, BHN, i, nb_); __builtin_amdgcn_sched_barrier(0); } \
    }                                                                           \
  } while (0)
  __syncthreads();
#pragma unroll
  for (int r = 0; r < 22; ++r) RD(afA, blA, bhA, r, smem);
#pragma unroll 1
  for (int kt = 0; kt < nst; kt += 2) {
    ITER(afA, blA, bhA, afB, blB, bhB, kt);
    ITER(afB, blB, bhB, afA, blA, bhA, kt + 1);
  }
  __syncthreads();
  {
    char* dst = smem + wave * 20480 + lane * 16;
#pragma unroll
    for (int n = 0; n < 9; ++n)
#pragma unroll
      for (int m = 0; m < 4; ++m) {
        bool mine = (wk == 0) ? (n < 5) : (n >= 5);
        int slot = (wk == 0) ? (n - 5) : n;
        if (!mine) *(f32x4*)(dst + (slot * 4 + m) * 1024) = acc[n][m];
      }
  }
  __syncthreads();
  {
    const char* s = smem + (wave ^ 2) * 20480 + lane * 16;
#pragma unroll
    for (int n = 0; n < 9; ++n)
#pragma unroll
      for (int m = 0; m < 4; ++m) {
        bool mine = (wk == 0) ? (n < 5) : (n >= 5);
        int slot = (wk == 0) ? n : (n - 5);
        if (mine) {
          f32x4 v = acc[n][m] + *(const f32x4*)(s + (slot * 4 + m) * 1024);
          size_t off = (size_t)(t0 + wt * 64 + m * 16 + fr) * D_ + e0 + n * 16 + fq * 4;
          if (sp < 2) {
            u16x4 w;
#pragma unroll
            for (int r = 0; r < 4; ++r) w[r] = f2bf(v[r]);
            *(u16x4*)((sp == 0 ? yre : ynim) + off) = w;
          } else {
            *(f32x4*)((sp == 2 ? olo : ohi) + off) = v;
          }
        }
      }
  }
}

__global__ __launch_bounds__(256) void k3_combine(const u16* __restrict__ x, const u16* __restrict__ yre,
                                                  const u16* __restrict__ ynim, const float* __restrict__ olo,
                                                  const float* __restrict__ ohi, u16* __restrict__ out) {
  const size_t i = ((size_t)blockIdx.x * 256 + threadIdx.x) * 8;
  u16x8 xv = *(const u16x8*)(x + i), a = *(const u16x8*)(yre + i), b = *(const u16x8*)(ynim + i);
  f32x4 l0 = *(const f32x4*)(olo + i), l1 = *(const f32x4*)(olo + i + 4);
  f32x4 h0 = *(const f32x4*)(ohi + i), h1 = *(const f32x4*)(ohi + i + 4);
  u16x8 r;
#pragma unroll
  for (int j = 0; j < 8; ++j) {
    float s = rbf(bf2f(a[j]) + bf2f(b[j]));
    float o = rbf((j < 4 ? l0[j] : l1[j - 4]) + (j < 4 ? h0[j] : h1[j - 4]));
    float g = rbf(o * sigmoidf_(o));
    r[j] = f2bf(bf2f(xv[j]) + rbf(s * g));
  }
  *(u16x8*)(out + i) = r;
}

extern "C" void kernel_launch(void* const* d_in, const int* in_sizes, int n_in,
                              void* d_out, int out_size, void* d_ws, size_t ws_size,
                              hipStream_t stream) {
  const u16* x = (const u16*)d_in[0];
  const u16* sre = (const u16*)d_in[1];
  const u16* sim = (const u16*)d_in[2];
  const u16* wre = (const u16*)d_in[3];
  const u16* wim = (const u16*)d_in[4];
  const u16* wg = (const u16*)d_in[6];
  const u16* wu = (const u16*)d_in[7];
  const u16* wd = (const u16*)d_in[8];
  char* ws = (char*)d_ws;
  const size_t SD2 = (size_t)S_ * D_ * 2;
  u16* xn = (u16*)(ws);
  u16* a = (u16*)(ws + SD2);
  u16* nb = (u16*)(ws + 2 * SD2);
  u16* yre = (u16*)(ws + 3 * SD2);
  u16* ynim = (u16*)(ws + 4 * SD2);
  float* olo = (float*)(ws + 5 * SD2);
  float* ohi = (float*)(ws + 7 * SD2);
  u16* h = (u16*)(ws + 9 * SD2);
  (void)hipFuncSetAttribute((const void*)k1_glu, hipFuncAttributeMaxDynamicSharedMemorySize, K1_LDS);
  (void)hipFuncSetAttribute((const void*)k2_gemm, hipFuncAttributeMaxDynamicSharedMemorySize, K2_LDS);
  k0_ln<<<S_ / 16, 256, 0, stream>>>(x, xn);
  k1_glu<<<256, 512, K1_LDS, stream>>>(xn, wg, wu, wre, wim, h, a, nb);
  k2_gemm<<<256, 512, K2_LDS, stream>>>(sre, sim, a, nb, h, wd, yre, ynim, olo, ohi);
  k3_combine<<<(S_ * D_ / 8) / 256, 256, 0, stream>>>(x, yre, ynim, olo, ohi, (u16*)d_out);
}
```

```cpp
#include <hip/hip_runtime.h>
#include <hip/hip_bf16.h>

typedef unsigned short u16;
typedef __attribute__((ext_vector_type(8))) short bf16x8;
typedef __attribute__((ext_vector_type(4))) short bf16x4;
typedef __attribute__((ext_vector_type(4))) float f32x4;
typedef __attribute__((ext_vector_type(4))) unsigned short u16x4;
typedef __attribute__((ext_vector_type(8))) unsigned short u16x8;
typedef __attribute__((address_space(3))) bf16x4 lds_bf16x4;

constexpr int S_ = 2048, D_ = 576, F_ = 4608;

__device__ __forceinline__ float bf2f(u16 v) { return __uint_as_float(((unsigned)v) << 16); }
__device__ __forceinline__ u16 f2bf(float f) { __bf16 b = (__bf16)f; return __builtin_bit_cast(u16, b); }
__device__ __forceinline__ float rbf(float f) { return bf2f(f2bf(f)); }
__device__ __forceinline__ float sigmoidf_(float x) {
  return __builtin_amdgcn_rcpf(1.0f + __builtin_amdgcn_exp2f(-1.4426950408889634f * x));
}

__device__ __forceinline__ void glds16s(const void* sbase, unsigned voff, unsigned lds_dst) {
  unsigned keep;
  asm volatile("s_mov_b32 %0, m0\n\ts_mov_b32 m0, %3\n\ts_nop 0\n\tglobal_load_lds_dwordx4 %1, %2\n\ts_mov_b32 m0, %0"
               : "=&s"(keep) : "v"(voff), "s"(sbase), "s"(lds_dst) : "memory");
}
__device__ __forceinline__ bf16x4 tr_read(const char* p) {
  return __builtin_amdgcn_ds_read_tr16_b64_v4i16((lds_bf16x4*)p);
}
__device__ __forceinline__ bf16x8 cat2(bf16x4 lo, bf16x4 hi) {
  return __builtin_shufflevector(lo, hi, 0, 1, 2, 3, 4, 5, 6, 7);
}
#define MFMA16(a, b, c) __builtin_amdgcn_mfma_f32_16x16x32_bf16((a), (b), (c), 0, 0, 0)
#define SEG(i)
#define SEGDECL
#define SEGOUT(cond, slot)
#define WAIT_V(n) asm volatile("s_waitcnt vmcnt(" #n ")" ::: "memory")
__device__ __forceinline__ int swz64(int row) { return (-(row >> 2)) & 3; }

__global__ __launch_bounds__(256) void k0_ln(const u16* __restrict__ x, u16* __restrict__ xn,
                                             const char* p0, const char* p1, const char* p2, const char* p3,
                                             const char* p4) {
  const int tid = threadIdx.x;
  if (blockIdx.x >= 128) {
    int b = blockIdx.x - 128;
    const char* p;
    if (b < 162) p = p0 + (size_t)b * 32768;
    else if (b < 324) p = p1 + (size_t)(b - 162) * 32768;
    else if (b < 580) p = p2 + (size_t)(b - 324) * 32768;
    else if (b < 836) p = p3 + (size_t)(b - 580) * 32768;
    else p = p4 + (size_t)(b - 836) * 32768;
    typedef __attribute__((ext_vector_type(4))) unsigned u32x4;
    u32x4 v[8];
#pragma unroll
    for (int j = 0; j < 8; ++j) v[j] = *(const u32x4*)(p + (size_t)(j * 256 + tid) * 16);
#pragma unroll
    for (int j = 0; j < 8; ++j) asm volatile("" ::"v"(v[j]));
    return;
  }
  const int row = blockIdx.x * 16 + (tid >> 4), l = tid & 15;
  const u16* xr = x + (size_t)row * D_ + l * 4;
  u16x4 v[9];
#pragma unroll
  for (int j = 0; j < 9; ++j) v[j] = *(const u16x4*)(xr + j * 64);
  float sum = 0.f;
#pragma unroll
  for (int j = 0; j < 9; ++j)
#pragma unroll
    for (int e = 0; e < 4; ++e) sum += bf2f(v[j][e]);
  sum += __shfl_xor(sum, 1); sum += __shfl_xor(sum, 2); sum += __shfl_xor(sum, 4); sum += __shfl_xor(sum, 8);
  const float mean = sum * (1.0f / D_);
  float vs = 0.f;
#pragma unroll
  for (int j = 0; j < 9; ++j)
#pragma unroll
    for (int e = 0; e < 4; ++e) { float d = bf2f(v[j][e]) - mean; vs += d * d; }
  vs += __shfl_xor(vs, 1); vs += __shfl_xor(vs, 2); vs += __shfl_xor(vs, 4); vs += __shfl_xor(vs, 8);
  const float mb = rbf(mean);
  const float vb = rbf(vs * (1.0f / D_));
  const float den = rbf(sqrtf(rbf(vb + 1e-5f)));
  const int r = row & 15;
  char* xo = (char*)xn + (size_t)(row >> 4) * 18 * 1024 + r * 64;
#pragma unroll
  for (int j = 0; j < 9; ++j) {
    u16x4 t;
#pragma unroll
    for (int e = 0; e < 4; ++e) t[e] = f2bf(rbf(bf2f(v[j][e]) - mb) / den);
    int col = j * 64 + l * 4;
    int kb = col >> 5, c = (col & 31) >> 3;
    *(u16x4*)(xo + kb * 1024 + ((c ^ swz64(r)) * 16) + (col & 7) * 2) = t;
  }
}

constexpr int K1_XS = 256 * 32 * 2;
constexpr int K1_GS = 32 * 144 * 2;
constexpr int K1_DS = 48 * 32 * 2;
constexpr int K1_SLOT = K1_XS + 2 * K1_GS + K1_DS;
constexpr int K1_LDS = 4 * K1_SLOT;

template <int NP, int NE, int PB, int EB>
__device__ __forceinline__ void k1_wave(char* smem, const u16* __restrict__ xn, const u16* __restrict__ wg,
                                        const u16* __restrict__ wu, const u16* __restrict__ wdf,
                                        u16* __restrict__ h, u16* __restrict__ a, u16* __restrict__ nb,
                                        int wave, int lane, int ft, int st) {
  SEGDECL
  const int ws = wave & 3;
  const int f0 = ft * 144, s0 = st * 256;
  const bool hasab = ft < 24;
  const int np = hasab ? 37 : 34;
  const int fr = lane & 15, fq = lane >> 4;
  unsigned voff[5];
#pragma unroll
  for (int j = 0; j < 5; ++j) {
    int i = wave + 8 * j;
    if (i < 16) {
      voff[j] = (unsigned)((s0 / 16 + i) * 18 * 1024 + lane * 16);
    } else if (i >= 34) {
      int r = ((i < 37) ? i - 34 : 0) * 16 + (lane >> 2);
      int c = (lane & 3) ^ swz64(r);
      voff[j] = (unsigned)(r * D_ + c * 8) * 2u;
    } else {
      int jj = (i - 16) % 9;
      int sb = jj * 4 + (lane >> 4), ii = lane & 15;
      int kb = sb / 9, nbk = sb % 9;
      int r = (ii >> 1) ^ ((kb & 1) << 2);
      voff[j] = (unsigned)((kb * 8 + r) * F_ + f0 + nbk * 16 + (ii & 1) * 8) * 2u;
    }
  }
  const bool p5 = wave + 32 < np;
  const unsigned lds0 = __builtin_amdgcn_readfirstlane((unsigned)(size_t)smem);
  auto piece = [&](int kt, int j) {
    int i = wave + 8 * j;
    const u16* base = (i < 16) ? (xn + kt * 512)
                    : (i < 34) ? ((i < 25 ? wg : wu) + (size_t)kt * 32 * F_) : (wdf + kt * 32);
    if (j < 4 || p5) glds16s(base, voff[j], lds0 + (kt & 3) * K1_SLOT + i * 1024);
  };
  f32x4 accg[NP][4] = {}, accu[NP][4] = {}, accd[NE][4] = {};
  bf16x8 xf[2][4], df[NE];
  bf16x4 gl[2], gh[2], ul[2], uh[2];
  const int q = (lane & 15) >> 2, p = lane & 3;
  const int sw = (fq & 1) << 2;
  const int xoff = (ws * 64 + fr) * 64 + ((fq ^ swz64(fr)) * 16);
  const int goff0 = K1_XS + (fq * 9 + PB) * 256 + p * 8 + ((q ^ sw) * 32);
  const int goff1 = K1_XS + (fq * 9 + PB) * 256 + p * 8 + (((q + 4) ^ sw) * 32);
  const int doff = K1_XS + 2 * K1_GS + (EB * 16 + fr) * 64 + ((fq ^ swz64(fr)) * 16);
#define K1_RDGU(bi, sb, t, r)                                                   \
  do {                                                                          \
    if ((r) == 0) gl[bi] = tr_read((sb) + goff0 + (t) * 256);                   \
    else if ((r) == 1) gh[bi] = tr_read((sb) + goff1 + (t) * 256);              \
    else if ((r) == 2) ul[bi] = tr_read((sb) + goff0 + (t) * 256 + K1_GS);      \
    else uh[bi] = tr_read((sb) + goff1 + (t) * 256 + K1_GS);                    \
  } while (0)
#define K1_FENCE __builtin_amdgcn_sched_barrier(0)
#define K1_STAGE(SP, KT)                                                        \
  do {                                                                          \
    SEG(4);                                                                     \
    if ((KT) + 2 < 18) { if (p5) WAIT_V(5); else WAIT_V(4); } else WAIT_V(0);   \
    SEG(2);                                                                     \
    __syncthreads();                                                            \
    SEG(3);                                                                     \
    const char* cb = smem + ((KT) & 3) * K1_SLOT;                               \
    const char* nx = smem + (((KT) + 1) & 3) * K1_SLOT;                         \
    const bool pf = (KT) + 3 < 18;                                              \
    _Pragma("unroll") for (int t = 0; t < NP; ++t) {                            \
      constexpr int dummy_ = 0; (void)dummy_;                                   \
      const int bc = ((SP) * NP + t) & 1, bn = bc ^ 1;                          \
      _Pragma("unroll") for (int i = 0; i < 8; ++i) {                           \
        const int m = i >> 1;                                                   \
        if ((i & 1) == 0) accg[t][m] = MFMA16(cat2(gl[bc], gh[bc]), xf[SP][m], accg[t][m]); \
        else accu[t][m] = MFMA16(cat2(ul[bc], uh[bc]), xf[SP][m], accu[t][m]);  \
        K1_FENCE;                                                               \
        if (i < 4) {                                                            \
          if (t + 1 < NP) K1_RDGU(bn, cb, t + 1, i); else K1_RDGU(bn, nx, 0, i); \
        } else if (t == NP - 1) {                                               \
          xf[(SP) ^ 1][i - 4] = *(const bf16x8*)(nx + xoff + (i - 4) * 1024);   \
        } else if (t == NP - 2 && i - 4 < NE) {                                 \
          df[i - 4] = *(const bf16x8*)(cb + doff + (i - 4) * 1024);             \
        } else if (i >= 6 && 2 * t + (i - 6) < 5) {                             \
          if (pf) piece((KT) + 3, 2 * t + (i - 6));                             \
        }                                                                       \
        K1_FENCE;                                                               \
      }                                                                         \
    }                                                                           \
    if (hasab) {                                                                \
      _Pragma("unroll") for (int e = 0; e < NE; ++e)                            \
        _Pragma("unroll") for (int m = 0; m < 4; ++m) accd[e][m] = MFMA16(df[e], xf[SP][m], accd[e][m]); \
    }                                                                           \
  } while (0)
#pragma unroll
  for (int s = 0; s < 3; ++s)
#pragma unroll
    for (int j = 0; j < 5; ++j) piece(s, j);
  SEG(0);
  if (p5) WAIT_V(10); else WAIT_V(8);
  __syncthreads();
  SEG(1);
#pragma unroll
  for (int m = 0; m < 4; ++m) xf[0][m] = *(const bf16x8*)(smem + xoff + m * 1024);
#pragma unroll
  for (int r = 0; r < 4; ++r) K1_RDGU(0, smem, 0, r);
#pragma unroll 1
  for (int kt = 0; kt < 18; kt += 2) {
    K1_STAGE(0, kt);
    K1_STAGE(1, kt + 1);
  }
  SEG(4);
#pragma unroll
  for (int t = 0; t < NP; ++t)
#pragma unroll
    for (int m = 0; m < 4; ++m) {
      u16x4 v;
#pragma unroll
      for (int r = 0; r < 4; ++r) {
        v[r] = f2bf(sigmoidf_(accg[t][m][r]) * accu[t][m][r]);
      }
      int srow = s0 + ws * 64 + m * 16 + fr;
      *(u16x4*)(h + (size_t)srow * F_ + f0 + (PB + t) * 16 + fq * 4) = v;
    }
  if (hasab) {
    const bool isim = ft >= 12;
    const float sg = isim ? -1.f : 1.f;
#pragma unroll
    for (int e = 0; e < NE; ++e) {
      const int ec = (isim ? ft - 12 : ft) * 48 + (EB + e) * 16;
      const int nt = ec / 144, nbk = (ec % 144) >> 4;
      char* o = (char*)(isim ? nb : a) + (size_t)nt * 32 * 18432 + nbk * 256 + fq * 8;
#pragma unroll
      for (int m = 0; m < 4; ++m) {
        u16x4 v;
#pragma unroll
        for (int r = 0; r < 4; ++r) v[r] = f2bf(sg * accd[e][m][r]);
        int s = s0 + ws * 64 + m * 16 + fr;
        int stg = s >> 6, kb = (s & 63) >> 3, rr = (s & 7) ^ ((kb & 1) << 2);
        *(u16x4*)(o + (size_t)stg * 18432 + kb * 2304 + rr * 32) = v;
      }
    }
  }
}

__global__ __launch_bounds__(512) void k1_glu(const u16* __restrict__ xn, const u16* __restrict__ wg,
                                              const u16* __restrict__ wu, const u16* __restrict__ wre,
                                              const u16* __restrict__ wim, u16* __restrict__ h,
                                              u16* __restrict__ a, u16* __restrict__ nb) {
  extern __shared__ __attribute__((aligned(16))) char smem[];
  const int tid = threadIdx.x, lane = tid & 63, wave = __builtin_amdgcn_readfirstlane(tid >> 6);
  const int xcd = blockIdx.x & 7, idx = blockIdx.x >> 3;
  const int ft = xcd * 4 + (idx >> 3), st = idx & 7;
  const u16* wdf = (ft < 12 ? wre + (size_t)ft * 48 * D_ : wim + (size_t)(ft < 24 ? ft - 12 : 0) * 48 * D_);
  if (wave < 4) k1_wave<5, 1, 0, 0>(smem, xn, wg, wu, wdf, h, a, nb, wave, lane, ft, st);
  else {
    __builtin_amdgcn_s_setprio(1);
    k1_wave<4, 2, 5, 1>(smem, xn, wg, wu, wdf, h, a, nb, wave, lane, ft, st);
  }
}

constexpr int K2_AS = 128 * 64 * 2;
constexpr int K2_BS = 64 * 144 * 2;
constexpr int K2_SLOT = K2_AS + K2_BS;
constexpr int K2_LDS = 4 * K2_SLOT;
__global__ __launch_bounds__(512) void k2_gemm(const u16* __restrict__ sre, const u16* __restrict__ sim,
                                               const u16* __restrict__ a, const u16* __restrict__ nb,
                                               const u16* __restrict__ h, const u16* __restrict__ wd,
                                               u16* __restrict__ yre, u16* __restrict__ ynim,
                                               float* __restrict__ olo, float* __restrict__ ohi) {
  extern __shared__ __attribute__((aligned(16))) char smem[];
  const int tid = threadIdx.x, lane = tid & 63, wave = __builtin_amdgcn_readfirstlane(tid >> 6);
  const int xcd = blockIdx.x & 7, idx = blockIdx.x >> 3;
  const int sp = xcd >> 1;
  const int mt = (xcd & 1) * 8 + (idx >> 2), nt = idx & 3;
  const int t0 = mt * 128, e0 = nt * 144;
  const int nst = sp < 2 ? 32 : 36;
  if (wave >= 4) {
    const int lw = wave - 4;
    const u16* A; const u16* B; int lda; int bstep;
    const bool img = sp < 2;
    if (sp == 0) { A = sre; B = a + (size_t)nt * 32 * 9216; lda = S_; bstep = 9216; }
    else if (sp == 1) { A = sim; B = nb + (size_t)nt * 32 * 9216; lda = S_; bstep = 9216; }
    else if (sp == 2) { A = h; B = wd; lda = F_; bstep = 64 * D_; }
    else { A = h + 2304; B = wd + (size_t)2304 * D_; lda = F_; bstep = 64 * D_; }
    unsigned voff[9];
#pragma unroll
    for (int j = 0; j < 9; ++j) {
      int i = lw + 4 * j;
      if (i < 16) {
        int r = i * 8 + (lane >> 3);
        int c = (lane & 7) ^ (lane >> 3);
        voff[j] = (unsigned)((t0 + r) * lda + c * 8) * 2u;
      } else {
        int jj = (i < 34) ? (i - 16) : 0;
        if (img) {
          voff[j] = (unsigned)(jj * 1024 + lane * 16);
        } else {
          int sb = jj * 4 + (lane >> 4), ii = lane & 15;
          int kb = sb / 9, nbk = sb % 9;
          int r = (ii >> 1) ^ ((kb & 1) << 2);
          voff[j] = (unsigned)((kb * 8 + r) * D_ + e0 + nbk * 16 + (ii & 1) * 8) * 2u;
        }
      }
    }
    const bool p9 = lw < 2;
    const unsigned lds0 = __builtin_amdgcn_readfirstlane((unsigned)(size_t)smem);
    auto stage = [&](int kt) {
#pragma unroll
      for (int j = 0; j < 9; ++j) {
        const u16* base = (j < 4) ? (A + kt * 64) : (B + (size_t)kt * bstep);
        if (j < 8 || p9) glds16s(base, voff[j], lds0 + (kt & 3) * K2_SLOT + (lw + 4 * j) * 1024);
      }
    };
    stage(0); stage(1); stage(2); stage(3);
    if (p9) WAIT_V(27); else WAIT_V(24);
    __syncthreads();
#pragma unroll 1
    for (int kt = 0; kt < nst; ++kt) {
      const int rem = nst - 2 - kt;
      if (rem >= 2) { if (p9) WAIT_V(18); else WAIT_V(16); }
      else if (rem == 1) { if (p9) WAIT_V(9); else WAIT_V(8); }
      else WAIT_V(0);
      __syncthreads();
      if (kt + 4 < nst) stage(kt + 4);
    }
    __syncthreads();
    __syncthreads();
    return;
  }
  const int wt = wave & 1, wk = wave >> 1;
  const int fr = lane & 15, fq = lane >> 4;
  const int q = (lane & 15) >> 2, p = lane & 3;
  const int sw = (fq & 1) << 2;
  f32x4 acc[9][4] = {};
  bf16x8 afA[4], afB[4];
  bf16x4 blA[9], bhA[9], blB[9], bhB[9];
  const int aoff = (wt * 64 + fr) * 128 + (((wk * 4 + fq) ^ (fr & 7)) * 16);
  const int boff0 = K2_AS + (wk * 4 + fq) * 9 * 256 + p * 8 + ((q ^ sw) * 32);
  const int boff1 = K2_AS + (wk * 4 + fq) * 9 * 256 + p * 8 + (((q + 4) ^ sw) * 32);
#define RD(AFN, BLN, BHN, r, base)                                              \
  do {                                                                          \
    if ((r) < 4) AFN[(r) & 3] = *(const bf16x8*)((base) + aoff + ((r) & 3) * 2048); \
    else if ((((r) - 4) & 1) == 0) BLN[((r) - 4) >> 1] = tr_read((base) + boff0 + (((r) - 4) >> 1) * 256); \
    else BHN[((r) - 4) >> 1] = tr_read((base) + boff1 + (((r) - 4) >> 1) * 256); \
  } while (0)
#define ITER(AFC, BLC, BHC, AFN, BLN, BHN, KT)                                  \
  do {                                                                          \
    __syncthreads();                                                            \
    const char* nb_ = smem + (((KT) + 1) & 3) * K2_SLOT;                        \
    _Pragma("unroll") for (int i = 0; i < 36; ++i) {                            \
      acc[i >> 2][i & 3] = MFMA16(cat2(BLC[i >> 2], BHC[i >> 2]), AFC[i & 3], acc[i >> 2][i & 3]); \
      __builtin_amdgcn_sched_barrier(0);                                        \
      if (i < 22) { RD(AFN, BLN, BHN, i, nb_); __builtin_amdgcn_sched_barrier(0); } \
    }                                                                           \
  } while (0)
  __syncthreads();
#pragma unroll
  for (int r = 0; r < 22; ++r) RD(afA, blA, bhA, r, smem);
#pragma unroll 1
  for (int kt = 0; kt < nst; kt += 2) {
    ITER(afA, blA, bhA, afB, blB, bhB, kt);
    ITER(afB, blB, bhB, afA, blA, bhA, kt + 1);
  }
  __syncthreads();
  {
    char* dst = smem + wave * 20480 + lane * 16;
#pragma unroll
    for (int n = 0; n < 9; ++n)
#pragma unroll
      for (int m = 0; m < 4; ++m) {
        bool mine = (wk == 0) ? (n < 5) : (n >= 5);
        int slot = (wk == 0) ? (n - 5) : n;
        if (!mine) *(f32x4*)(dst + (slot * 4 + m) * 1024) = acc[n][m];
      }
  }
  __syncthreads();
  {
    const char* s = smem + (wave ^ 2) * 20480 + lane * 16;
#pragma unroll
    for (int n = 0; n < 9; ++n)
#pragma unroll
      for (int m = 0; m < 4; ++m) {
        bool mine = (wk == 0) ? (n < 5) : (n >= 5);
        int slot = (wk == 0) ? n : (n - 5);
        if (mine) {
          f32x4 v = acc[n][m] + *(const f32x4*)(s + (slot * 4 + m) * 1024);
          size_t off = (size_t)(t0 + wt * 64 + m * 16 + fr) * D_ + e0 + n * 16 + fq * 4;
          if (sp < 2) {
            u16x4 w;
#pragma unroll
            for (int r = 0; r < 4; ++r) w[r] = f2bf(v[r]);
            *(u16x4*)((sp == 0 ? yre : ynim) + off) = w;
          } else {
            *(f32x4*)((sp == 2 ? olo : ohi) + off) = v;
          }
        }
      }
  }
}

__global__ __launch_bounds__(256) void k3_combine(const u16* __restrict__ x, const u16* __restrict__ yre,
                                                  const u16* __restrict__ ynim, const float* __restrict__ olo,
                                                  const float* __restrict__ ohi, u16* __restrict__ out) {
  const size_t i = ((size_t)blockIdx.x * 256 + threadIdx.x) * 8;
  u16x8 xv = *(const u16x8*)(x + i), a = *(const u16x8*)(yre + i), b = *(const u16x8*)(ynim + i);
  f32x4 l0 = *(const f32x4*)(olo + i), l1 = *(const f32x4*)(olo + i + 4);
  f32x4 h0 = *(const f32x4*)(ohi + i), h1 = *(const f32x4*)(ohi + i + 4);
  u16x8 r;
#pragma unroll
  for (int j = 0; j < 8; ++j) {
    float s = rbf(bf2f(a[j]) + bf2f(b[j]));
    float o = rbf((j < 4 ? l0[j] : l1[j - 4]) + (j < 4 ? h0[j] : h1[j - 4]));
    float g = rbf(o * sigmoidf_(o));
    r[j] = f2bf(bf2f(xv[j]) + rbf(s * g));
  }
  *(u16x8*)(out + i) = r;
}

extern "C" void kernel_launch(void* const* d_in, const int* in_sizes, int n_in,
                              void* d_out, int out_size, void* d_ws, size_t ws_size,
                              hipStream_t stream) {
  const u16* x = (const u16*)d_in[0];
  const u16* sre = (const u16*)d_in[1];
  const u16* sim = (const u16*)d_in[2];
  const u16* wre = (const u16*)d_in[3];
  const u16* wim = (const u16*)d_in[4];
  const u16* wg = (const u16*)d_in[6];
  const u16* wu = (const u16*)d_in[7];
  const u16* wd = (const u16*)d_in[8];
  char* ws = (char*)d_ws;
  const size_t SD2 = (size_t)S_ * D_ * 2;
  u16* xn = (u16*)(ws);
  u16* a = (u16*)(ws + SD2);
  u16* nb = (u16*)(ws + 2 * SD2);
  u16* yre = (u16*)(ws + 3 * SD2);
  u16* ynim = (u16*)(ws + 4 * SD2);
  float* olo = (float*)(ws + 5 * SD2);
  float* ohi = (float*)(ws + 7 * SD2);
  u16* h = (u16*)(ws + 9 * SD2);
  (void)hipFuncSetAttribute((const void*)k1_glu, hipFuncAttributeMaxDynamicSharedMemorySize, K1_LDS);
  (void)hipFuncSetAttribute((const void*)k2_gemm, hipFuncAttributeMaxDynamicSharedMemorySize, K2_LDS);
  k0_ln<<<128 + 998, 256, 0, stream>>>(x, xn, (const char*)wg, (const char*)wu, (const char*)sre, (const char*)sim, (const char*)wd);
  k1_glu<<<256, 512, K1_LDS, stream>>>(xn, wg, wu, wre, wim, h, a, nb);
  k2_gemm<<<256, 512, K2_LDS, stream>>>(sre, sim, a, nb, h, wd, yre, ynim, olo, ohi);
  k3_combine<<<(S_ * D_ / 8) / 256, 256, 0, stream>>>(x, yre, ynim, olo, ohi, (u16*)d_out);
}
```

```cpp
#include <hip/hip_runtime.h>
#include <hip/hip_bf16.h>

typedef unsigned short u16;
typedef __attribute__((ext_vector_type(8))) short bf16x8;
typedef __attribute__((ext_vector_type(4))) short bf16x4;
typedef __attribute__((ext_vector_type(4))) float f32x4;
typedef __attribute__((ext_vector_type(4))) unsigned short u16x4;
typedef __attribute__((ext_vector_type(8))) unsigned short u16x8;
typedef __attribute__((address_space(3))) bf16x4 lds_bf16x4;

constexpr int S_ = 2048, D_ = 576, F_ = 4608;

__device__ __forceinline__ float bf2f(u16 v) { return __uint_as_float(((unsigned)v) << 16); }
__device__ __forceinline__ u16 f2bf(float f) { __bf16 b = (__bf16)f; return __builtin_bit_cast(u16, b); }
__device__ __forceinline__ float rbf(float f) { return bf2f(f2bf(f)); }
__device__ __forceinline__ float sigmoidf_(float x) {
  return __builtin_amdgcn_rcpf(1.0f + __builtin_amdgcn_exp2f(-1.4426950408889634f * x));
}

__device__ __forceinline__ void glds16s(const void* sbase, unsigned voff, unsigned lds_dst) {
  unsigned keep;
  asm volatile("s_mov_b32 %0, m0\n\ts_mov_b32 m0, %3\n\ts_nop 0\n\tglobal_load_lds_dwordx4 %1, %2\n\ts_mov_b32 m0, %0"
               : "=&s"(keep) : "v"(voff), "s"(sbase), "s"(lds_dst) : "memory");
}
__device__ __forceinline__ bf16x4 tr_read(const char* p) {
  return __builtin_amdgcn_ds_read_tr16_b64_v4i16((lds_bf16x4*)p);
}
__device__ __forceinline__ bf16x8 cat2(bf16x4 lo, bf16x4 hi) {
  return __builtin_shufflevector(lo, hi, 0, 1, 2, 3, 4, 5, 6, 7);
}
#define MFMA16(a, b, c) __builtin_amdgcn_mfma_f32_16x16x32_bf16((a), (b), (c), 0, 0, 0)
#define SEG(i)
#define SEGDECL
#define SEGOUT(cond, slot)
#define WAIT_V(n) asm volatile("s_waitcnt vmcnt(" #n ")" ::: "memory")
__device__ __forceinline__ int swz64(int row) { return (-(row >> 2)) & 3; }

__global__ __launch_bounds__(256) void k0_ln(const u16* __restrict__ x, u16* __restrict__ xn,
                                             const char* p0, const char* p1) {
  const int tid = threadIdx.x;
  if (blockIdx.x >= 128) {
    int b = blockIdx.x - 128;
    const char* p;
    if (b < 162) p = p0 + (size_t)b * 32768;
    else p = p1 + (size_t)(b - 162) * 32768;
    typedef __attribute__((ext_vector_type(4))) unsigned u32x4;
    u32x4 v[8];
#pragma unroll
    for (int j = 0; j < 8; ++j) v[j] = *(const u32x4*)(p + (size_t)(j * 256 + tid) * 16);
#pragma unroll
    for (int j = 0; j < 8; ++j) asm volatile("" ::"v"(v[j]));
    return;
  }
  const int row = blockIdx.x * 16 + (tid >> 4), l = tid & 15;
  const u16* xr = x + (size_t)row * D_ + l * 4;
  u16x4 v[9];
#pragma unroll
  for (int j = 0; j < 9; ++j) v[j] = *(const u16x4*)(xr + j * 64);
  float sum = 0.f;
#pragma unroll
  for (int j = 0; j < 9; ++j)
#pragma unroll
    for (int e = 0; e < 4; ++e) sum += bf2f(v[j][e]);
  sum += __shfl_xor(sum, 1); sum += __shfl_xor(sum, 2); sum += __shfl_xor(sum, 4); sum += __shfl_xor(sum, 8);
  const float mean = sum * (1.0f / D_);
  float vs = 0.f;
#pragma unroll
  for (int j = 0; j < 9; ++j)
#pragma unroll
    for (int e = 0; e < 4; ++e) { float d = bf2f(v[j][e]) - mean; vs += d * d; }
  vs += __shfl_xor(vs, 1); vs += __shfl_xor(vs, 2); vs += __shfl_xor(vs, 4); vs += __shfl_xor(vs, 8);
  const float mb = rbf(mean);
  const float vb = rbf(vs * (1.0f / D_));
  const float den = rbf(sqrtf(rbf(vb + 1e-5f)));
  const int r = row & 15;
  char* xo = (char*)xn + (size_t)(row >> 4) * 18 * 1024 + r * 64;
#pragma unroll
  for (int j = 0; j < 9; ++j) {
    u16x4 t;
#pragma unroll
    for (int e = 0; e < 4; ++e) t[e] = f2bf(rbf(bf2f(v[j][e]) - mb) / den);
    int col = j * 64 + l * 4;
    int kb = col >> 5, c = (col & 31) >> 3;
    *(u16x4*)(xo + kb * 1024 + ((c ^ swz64(r)) * 16) + (col & 7) * 2) = t;
  }
}

constexpr int K1_XS = 256 * 32 * 2;
constexpr int K1_GS = 32 * 144 * 2;
constexpr int K1_DS = 48 * 32 * 2;
constexpr int K1_SLOT = K1_XS + 2 * K1_GS + K1_DS;
constexpr int K1_LDS = 4 * K1_SLOT + 8192;

template <int NP, int NE, int PB, int EB>
__device__ __forceinline__ void k1_wave(char* smem, const u16* __restrict__ xn, const u16* __restrict__ wg,
                                        const u16* __restrict__ wu, const u16* __restrict__ wdf,
                                        u16* __restrict__ h, u16* __restrict__ a, u16* __restrict__ nb,
                                        const char* sre, const char* sim, const char* wd,
                                        int wave, int lane, int ft, int st) {
  SEGDECL
  const int ws = wave & 3;
  const int f0 = ft * 144, s0 = st * 256;
  const bool hasab = ft < 24;
  const int np = hasab ? 37 : 34;
  const int fr = lane & 15, fq = lane >> 4;
  unsigned voff[5];
#pragma unroll
  for (int j = 0; j < 5; ++j) {
    int i = wave + 8 * j;
    if (i < 16) {
      voff[j] = (unsigned)((s0 / 16 + i) * 18 * 1024 + lane * 16);
    } else if (i >= 34) {
      int r = ((i < 37) ? i - 34 : 0) * 16 + (lane >> 2);
      int c = (lane & 3) ^ swz64(r);
      voff[j] = (unsigned)(r * D_ + c * 8) * 2u;
    } else {
      int jj = (i - 16) % 9;
      int sb = jj * 4 + (lane >> 4), ii = lane & 15;
      int kb = sb / 9, nbk = sb % 9;
      int r = (ii >> 1) ^ ((kb & 1) << 2);
      voff[j] = (unsigned)((kb * 8 + r) * F_ + f0 + nbk * 16 + (ii & 1) * 8) * 2u;
    }
  }
  const bool p5 = wave + 32 < np;
  const unsigned lds0 = __builtin_amdgcn_readfirstlane((unsigned)(size_t)smem);
  auto piece = [&](int kt, int j) {
    int i = wave + 8 * j;
    const u16* base = (i < 16) ? (xn + kt * 512)
                    : (i < 34) ? ((i < 25 ? wg : wu) + (size_t)kt * 32 * F_) : (wdf + kt * 32);
    if (j < 4 || p5) glds16s(base, voff[j], lds0 + (kt & 3) * K1_SLOT + i * 1024);
  };
  auto warm = [&](int n) {
    int kib = blockIdx.x * 85 + n * 8 + wave;
    kib = (n * 8 + wave < 85 && kib < 21568) ? kib : 0;
    const char* base = kib < 8192 ? sre + (size_t)kib * 1024
                     : kib < 16384 ? sim + (size_t)(kib - 8192) * 1024 : wd + (size_t)(kib - 16384) * 1024;
    glds16s(base, lane * 16, lds0 + 4 * K1_SLOT + wave * 1024);
  };
  f32x4 accg[NP][4] = {}, accu[NP][4] = {}, accd[NE][4] = {};
  bf16x8 xf[2][4], df[NE];
  bf16x4 gl[2], gh[2], ul[2], uh[2];
  const int q = (lane & 15) >> 2, p = lane & 3;
  const int sw = (fq & 1) << 2;
  const int xoff = (ws * 64 + fr) * 64 + ((fq ^ swz64(fr)) * 16);
  const int goff0 = K1_XS + (fq * 9 + PB) * 256 + p * 8 + ((q ^ sw) * 32);
  const int goff1 = K1_XS + (fq * 9 + PB) * 256 + p * 8 + (((q + 4) ^ sw) * 32);
  const int doff = K1_XS + 2 * K1_GS + (EB * 16 + fr) * 64 + ((fq ^ swz64(fr)) * 16);
#define K1_RDGU(bi, sb, t, r)                                                   \
  do {                                                                          \
    if ((r) == 0) gl[bi] = tr_read((sb) + goff0 + (t) * 256);                   \
    else if ((r) == 1) gh[bi] = tr_read((sb) + goff1 + (t) * 256);              \
    else if ((r) == 2) ul[bi] = tr_read((sb) + goff0 + (t) * 256 + K1_GS);      \
    else uh[bi] = tr_read((sb) + goff1 + (t) * 256 + K1_GS);                    \
  } while (0)
#define K1_FENCE __builtin_amdgcn_sched_barrier(0)
#define K1_STAGE(SP, KT)                                                        \
  do {                                                                          \
    SEG(4);                                                                     \
    if ((KT) + 2 < 18) {                                                        \
      if ((KT) == 0) { if (p5) WAIT_V(5); else WAIT_V(4); }                     \
      else { if (p5) WAIT_V(6); else WAIT_V(5); }                               \
    } else WAIT_V(0);                                                           \
    SEG(2);                                                                     \
    __syncthreads();                                                            \
    SEG(3);                                                                     \
    const char* cb = smem + ((KT) & 3) * K1_SLOT;                               \
    const char* nx = smem + (((KT) + 1) & 3) * K1_SLOT;                         \
    const bool pf = (KT) + 3 < 18;                                              \
    _Pragma("unroll") for (int t = 0; t < NP; ++t) {                            \
      constexpr int dummy_ = 0; (void)dummy_;                                   \
      const int bc = ((SP) * NP + t) & 1, bn = bc ^ 1;                          \
      _Pragma("unroll") for (int i = 0; i < 8; ++i) {                           \
        const int m = i >> 1;                                                   \
        if ((i & 1) == 0) accg[t][m] = MFMA16(cat2(gl[bc], gh[bc]), xf[SP][m], accg[t][m]); \
        else accu[t][m] = MFMA16(cat2(ul[bc], uh[bc]), xf[SP][m], accu[t][m]);  \
        K1_FENCE;                                                               \
        if (i < 4) {                                                            \
          if (t + 1 < NP) K1_RDGU(bn, cb, t + 1, i); else K1_RDGU(bn, nx, 0, i); \
        } else if (t == NP - 1) {                                               \
          xf[(SP) ^ 1][i - 4] = *(const bf16x8*)(nx + xoff + (i - 4) * 1024);   \
        } else if (t == NP - 2 && i - 4 < NE) {                                 \
          df[i - 4] = *(const bf16x8*)(cb + doff + (i - 4) * 1024);             \
        } else if (i >= 6 && 2 * t + (i - 6) < 5) {                             \
          if (pf) piece((KT) + 3, 2 * t + (i - 6));                             \
        } else if (i == 7 && t == 2) {                                          \
          if (pf) warm(KT);                                                     \
        }                                                                       \
        K1_FENCE;                                                               \
      }                                                                         \
    }                                                                           \
    if (hasab) {                                                                \
      _Pragma("unroll") for (int e = 0; e < NE; ++e)                            \
        _Pragma("unroll") for (int m = 0; m < 4; ++m) accd[e][m] = MFMA16(df[e], xf[SP][m], accd[e][m]); \
    }                                                                           \
  } while (0)
#pragma unroll
  for (int s = 0; s < 3; ++s)
#pragma unroll
    for (int j = 0; j < 5; ++j) piece(s, j);
  SEG(0);
  if (p5) WAIT_V(10); else WAIT_V(8);
  __syncthreads();
  SEG(1);
#pragma unroll
  for (int m = 0; m < 4; ++m) xf[0][m] = *(const bf16x8*)(smem + xoff + m * 1024);
#pragma unroll
  for (int r = 0; r < 4; ++r) K1_RDGU(0, smem, 0, r);
#pragma unroll 1
  for (int kt = 0; kt < 18; kt += 2) {
    K1_STAGE(0, kt);
    K1_STAGE(1, kt + 1);
  }
  SEG(4);
#pragma unroll
  for (int t = 0; t < NP; ++t)
#pragma unroll
    for (int m = 0; m < 4; ++m) {
      u16x4 v;
#pragma unroll
      for (int r = 0; r < 4; ++r) {
        v[r] = f2bf(sigmoidf_(accg[t][m][r]) * accu[t][m][r]);
      }
      int srow = s0 + ws * 64 + m * 16 + fr;
      *(u16x4*)(h + (size_t)srow * F_ + f0 + (PB + t) * 16 + fq * 4) = v;
    }
  if (hasab) {
    const bool isim = ft >= 12;
    const float sg = isim ? -1.f : 1.f;
#pragma unroll
    for (int e = 0; e < NE; ++e) {
      const int ec = (isim ? ft - 12 : ft) * 48 + (EB + e) * 16;
      const int nt = ec / 144, nbk = (ec % 144) >> 4;
      char* o = (char*)(isim ? nb : a) + (size_t)nt * 32 * 18432 + nbk * 256 + fq * 8;
#pragma unroll
      for (int m = 0; m < 4; ++m) {
        u16x4 v;
#pragma unroll
        for (int r = 0; r < 4; ++r) v[r] = f2bf(sg * accd[e][m][r]);
        int s = s0 + ws * 64 + m * 16 + fr;
        int stg = s >> 6, kb = (s & 63) >> 3, rr = (s & 7) ^ ((kb & 1) << 2);
        *(u16x4*)(o + (size_t)stg * 18432 + kb * 2304 + rr * 32) = v;
      }
    }
  }
}

__global__ __launch_bounds__(512) void k1_glu(const u16* __restrict__ xn, const u16* __restrict__ wg,
                                              const u16* __restrict__ wu, const u16* __restrict__ wre,
                                              const u16* __restrict__ wim, u16* __restrict__ h,
                                              u16* __restrict__ a, u16* __restrict__ nb,
                                              const char* sre, const char* sim, const char* wd) {
  extern __shared__ __attribute__((aligned(16))) char smem[];
  const int tid = threadIdx.x, lane = tid & 63, wave = __builtin_amdgcn_readfirstlane(tid >> 6);
  const int xcd = blockIdx.x & 7, idx = blockIdx.x >> 3;
  const int ft = xcd * 4 + (idx >> 3), st = idx & 7;
  const u16* wdf = (ft < 12 ? wre + (size_t)ft * 48 * D_ : wim + (size_t)(ft < 24 ? ft - 12 : 0) * 48 * D_);
  if (wave < 4) k1_wave<5, 1, 0, 0>(smem, xn, wg, wu, wdf, h, a, nb, sre, sim, wd, wave, lane, ft, st);
  else {
    __builtin_amdgcn_s_setprio(1);
    k1_wave<4, 2, 5, 1>(smem, xn, wg, wu, wdf, h, a, nb, sre, sim, wd, wave, lane, ft, st);
  }
}

constexpr int K2_AS = 128 * 64 * 2;
constexpr int K2_BS = 64 * 144 * 2;
constexpr int K2_SLOT = K2_AS + K2_BS;
constexpr int K2_LDS = 4 * K2_SLOT;
__global__ __launch_bounds__(512) void k2_gemm(const u16* __restrict__ sre, const u16* __restrict__ sim,
                                               const u16* __restrict__ a, const u16* __restrict__ nb,
                                               const u16* __restrict__ h, const u16* __restrict__ wd,
                                               u16* __restrict__ yre, u16* __restrict__ ynim,
                                               float* __restrict__ olo, float* __restrict__ ohi) {
  extern __shared__ __attribute__((aligned(16))) char smem[];
  const int tid = threadIdx.x, lane = tid & 63, wave = __builtin_amdgcn_readfirstlane(tid >> 6);
  const int xcd = blockIdx.x & 7, idx = blockIdx.x >> 3;
  const int sp = xcd >> 1;
  const int mt = (xcd & 1) * 8 + (idx >> 2), nt = idx & 3;
  const int t0 = mt * 128, e0 = nt * 144;
  const int nst = sp < 2 ? 32 : 36;
  if (wave >= 4) {
    const int lw = wave - 4;
    const u16* A; const u16* B; int lda; int bstep;
    const bool img = sp < 2;
    if (sp == 0) { A = sre; B = a + (size_t)nt * 32 * 9216; lda = S_; bstep = 9216; }
    else if (sp == 1) { A = sim; B = nb + (size_t)nt * 32 * 9216; lda = S_; bstep = 9216; }
    else if (sp == 2) { A = h; B = wd; lda = F_; bstep = 64 * D_; }
    else { A = h + 2304; B = wd + (size_t)2304 * D_; lda = F_; bstep = 64 * D_; }
    unsigned voff[9];
#pragma unroll
    for (int j = 0; j < 9; ++j) {
      int i = lw + 4 * j;
      if (i < 16) {
        int r = i * 8 + (lane >> 3);
        int c = (lane & 7) ^ (lane >> 3);
        voff[j] = (unsigned)((t0 + r) * lda + c * 8) * 2u;
      } else {
        int jj = (i < 34) ? (i - 16) : 0;
        if (img) {
          voff[j] = (unsigned)(jj * 1024 + lane * 16);
        } else {
          int sb = jj * 4 + (lane >> 4), ii = lane & 15;
          int kb = sb / 9, nbk = sb % 9;
          int r = (ii >> 1) ^ ((kb & 1) << 2);
          voff[j] = (unsigned)((kb * 8 + r) * D_ + e0 + nbk * 16 + (ii & 1) * 8) * 2u;
        }
      }
    }
    const bool p9 = lw < 2;
    const unsigned lds0 = __builtin_amdgcn_readfirstlane((unsigned)(size_t)smem);
    auto stage = [&](int kt) {
#pragma unroll
      for (int j = 0; j < 9; ++j) {
        const u16* base = (j < 4) ? (A + kt * 64) : (B + (size_t)kt * bstep);
        if (j < 8 || p9) glds16s(base, voff[j], lds0 + (kt & 3) * K2_SLOT + (lw + 4 * j) * 1024);
      }
    };
    stage(0); stage(1); stage(2); stage(3);
    if (p9) WAIT_V(27); else WAIT_V(24);
    __syncthreads();
#pragma unroll 1
    for (int kt = 0; kt < nst; ++kt) {
      const int rem = nst - 2 - kt;
      if (rem >= 2) { if (p9) WAIT_V(18); else WAIT_V(16); }
      else if (rem == 1) { if (p9) WAIT_V(9); else WAIT_V(8); }
      else WAIT_V(0);
      __syncthreads();
      if (kt + 4 < nst) stage(kt + 4);
    }
    __syncthreads();
    __syncthreads();
    return;
  }
  const int wt = wave & 1, wk = wave >> 1;
  const int fr = lane & 15, fq = lane >> 4;
  const int q = (lane & 15) >> 2, p = lane & 3;
  const int sw = (fq & 1) << 2;
  f32x4 acc[9][4] = {};
  bf16x8 afA[4], afB[4];
  bf16x4 blA[9], bhA[9], blB[9], bhB[9];
  const int aoff = (wt * 64 + fr) * 128 + (((wk * 4 + fq) ^ (fr & 7)) * 16);
  const int boff0 = K2_AS + (wk * 4 + fq) * 9 * 256 + p * 8 + ((q ^ sw) * 32);
  const int boff1 = K2_AS + (wk * 4 + fq) * 9 * 256 + p * 8 + (((q + 4) ^ sw) * 32);
#define RD(AFN, BLN, BHN, r, base)                                              \
  do {                                                                          \
    if ((r) < 4) AFN[(r) & 3] = *(const bf16x8*)((base) + aoff + ((r) & 3) * 2048); \
    else if ((((r) - 4) & 1) == 0) BLN[((r) - 4) >> 1] = tr_read((base) + boff0 + (((r) - 4) >> 1) * 256); \
    else BHN[((r) - 4) >> 1] = tr_read((base) + boff1 + (((r) - 4) >> 1) * 256); \
  } while (0)
#define ITER(AFC, BLC, BHC, AFN, BLN, BHN, KT)                                  \
  do {                                                                          \
    __syncthreads();                                                            \
    const char* nb_ = smem + (((KT) + 1) & 3) * K2_SLOT;                        \
    _Pragma("unroll") for (int i = 0; i < 36; ++i) {                            \
      acc[i >> 2][i & 3] = MFMA16(cat2(BLC[i >> 2], BHC[i >> 2]), AFC[i & 3], acc[i >> 2][i & 3]); \
      __builtin_amdgcn_sched_barrier(0);                                        \
      if (i < 22) { RD(AFN, BLN, BHN, i, nb_); __builtin_amdgcn_sched_barrier(0); } \
    }                                                                           \
  } while (0)
  __syncthreads();
#pragma unroll
  for (int r = 0; r < 22; ++r) RD(afA, blA, bhA, r, smem);
#pragma unroll 1
  for (int kt = 0; kt < nst; kt += 2) {
    ITER(afA, blA, bhA, afB, blB, bhB, kt);
    ITER(afB, blB, bhB, afA, blA, bhA, kt + 1);
  }
  __syncthreads();
  {
    char* dst = smem + wave * 20480 + lane * 16;
#pragma unroll
    for (int n = 0; n < 9; ++n)
#pragma unroll
      for (int m = 0; m < 4; ++m) {
        bool mine = (wk == 0) ? (n < 5) : (n >= 5);
        int slot = (wk == 0) ? (n - 5) : n;
        if (!mine) *(f32x4*)(dst + (slot * 4 + m) * 1024) = acc[n][m];
      }
  }
  __syncthreads();
  {
    const char* s = smem + (wave ^ 2) * 20480 + lane * 16;
#pragma unroll
    for (int n = 0; n < 9; ++n)
#pragma unroll
      for (int m = 0; m < 4; ++m) {
        bool mine = (wk == 0) ? (n < 5) : (n >= 5);
        int slot = (wk == 0) ? n : (n - 5);
        if (mine) {
          f32x4 v = acc[n][m] + *(const f32x4*)(s + (slot * 4 + m) * 1024);
          size_t off = (size_t)(t0 + wt * 64 + m * 16 + fr) * D_ + e0 + n * 16 + fq * 4;
          if (sp < 2) {
            u16x4 w;
#pragma unroll
            for (int r = 0; r < 4; ++r) w[r] = f2bf(v[r]);
            *(u16x4*)((sp == 0 ? yre : ynim) + off) = w;
          } else {
            *(f32x4*)((sp == 2 ? olo : ohi) + off) = v;
          }
        }
      }
  }
}

__global__ __launch_bounds__(256) void k3_combine(const u16* __restrict__ x, const u16* __restrict__ yre,
                                                  const u16* __restrict__ ynim, const float* __restrict__ olo,
                                                  const float* __restrict__ ohi, u16* __restrict__ out) {
  const size_t i = ((size_t)blockIdx.x * 256 + threadIdx.x) * 8;
  u16x8 xv = *(const u16x8*)(x + i), a = *(const u16x8*)(yre + i), b = *(const u16x8*)(ynim + i);
  f32x4 l0 = *(const f32x4*)(olo + i), l1 = *(const f32x4*)(olo + i + 4);
  f32x4 h0 = *(const f32x4*)(ohi + i), h1 = *(const f32x4*)(ohi + i + 4);
  u16x8 r;
#pragma unroll
  for (int j = 0; j < 8; ++j) {
    float s = rbf(bf2f(a[j]) + bf2f(b[j]));
    float o = rbf((j < 4 ? l0[j] : l1[j - 4]) + (j < 4 ? h0[j] : h1[j - 4]));
    float g = rbf(o * sigmoidf_(o));
    r[j] = f2bf(bf2f(xv[j]) + rbf(s * g));
  }
  *(u16x8*)(out + i) = r;
}

extern "C" void kernel_launch(void* const* d_in, const int* in_sizes, int n_in,
                              void* d_out, int out_size, void* d_ws, size_t ws_size,
                              hipStream_t stream) {
  const u16* x = (const u16*)d_in[0];
  const u16* sre = (const u16*)d_in[1];
  const u16* sim = (const u16*)d_in[2];
  const u16* wre = (const u16*)d_in[3];
  const u16* wim = (const u16*)d_in[4];
  const u16* wg = (const u16*)d_in[6];
  const u16* wu = (const u16*)d_in[7];
  const u16* wd = (const u16*)d_in[8];
  char* ws = (char*)d_ws;
  const size_t SD2 = (size_t)S_ * D_ * 2;
  u16* xn = (u16*)(ws);
  u16* a = (u16*)(ws + SD2);
  u16* nb = (u16*)(ws + 2 * SD2);
  u16* yre = (u16*)(ws + 3 * SD2);
  u16* ynim = (u16*)(ws + 4 * SD2);
  float* olo = (float*)(ws + 5 * SD2);
  float* ohi = (float*)(ws + 7 * SD2);
  u16* h = (u16*)(ws + 9 * SD2);
  (void)hipFuncSetAttribute((const void*)k1_glu, hipFuncAttributeMaxDynamicSharedMemorySize, K1_LDS);
  (void)hipFuncSetAttribute((const void*)k2_gemm, hipFuncAttributeMaxDynamicSharedMemorySize, K2_LDS);
  k0_ln<<<128 + 324, 256, 0, stream>>>(x, xn, (const char*)wg, (const char*)wu);
  k1_glu<<<256, 512, K1_LDS, stream>>>(xn, wg, wu, wre, wim, h, a, nb, (const char*)sre, (const char*)sim, (const char*)wd);
  k2_gemm<<<256, 512, K2_LDS, stream>>>(sre, sim, a, nb, h, wd, yre, ynim, olo, ohi);
  k3_combine<<<(S_ * D_ / 8) / 256, 256, 0, stream>>>(x, yre, ynim, olo, ohi, (u16*)d_out);
}
```

```cpp
#include <hip/hip_runtime.h>
#include <hip/hip_bf16.h>

typedef unsigned short u16;
typedef __attribute__((ext_vector_type(8))) short bf16x8;
typedef __attribute__((ext_vector_type(4))) short bf16x4;
typedef __attribute__((ext_vector_type(4))) float f32x4;
typedef __attribute__((ext_vector_type(4))) unsigned short u16x4;
typedef __attribute__((ext_vector_type(8))) unsigned short u16x8;
typedef __attribute__((address_space(3))) bf16x4 lds_bf16x4;

constexpr int S_ = 2048, D_ = 576, F_ = 4608;

__device__ __forceinline__ float bf2f(u16 v) { return __uint_as_float(((unsigned)v) << 16); }
__device__ __forceinline__ u16 f2bf(float f) { __bf16 b = (__bf16)f; return __builtin_bit_cast(u16, b); }
__device__ __forceinline__ float rbf(float f) { return bf2f(f2bf(f)); }
__device__ __forceinline__ float sigmoidf_(float x) {
  return __builtin_amdgcn_rcpf(1.0f + __builtin_amdgcn_exp2f(-1.4426950408889634f * x));
}

__device__ __forceinline__ void glds16s(const void* sbase, unsigned voff, unsigned lds_dst) {
  unsigned keep;
  asm volatile("s_mov_b32 %0, m0\n\ts_mov_b32 m0, %3\n\ts_nop 0\n\tglobal_load_lds_dwordx4 %1, %2\n\ts_mov_b32 m0, %0"
               : "=&s"(keep) : "v"(voff), "s"(sbase), "s"(lds_dst) : "memory");
}
__device__ __forceinline__ bf16x4 tr_read(const char* p) {
  return __builtin_amdgcn_ds_read_tr16_b64_v4i16((lds_bf16x4*)p);
}
__device__ __forceinline__ bf16x8 cat2(bf16x4 lo, bf16x4 hi) {
  return __builtin_shufflevector(lo, hi, 0, 1, 2, 3, 4, 5, 6, 7);
}
#define MFMA16(a, b, c) __builtin_amdgcn_mfma_f32_16x16x32_bf16((a), (b), (c), 0, 0, 0)
#define SEG(i)
#define SEGDECL
#define SEGOUT(cond, slot)
#define WAIT_V(n) asm volatile("s_waitcnt vmcnt(" #n ")" ::: "memory")
__device__ __forceinline__ int swz64(int row) { return (-(row >> 2)) & 3; }

__global__ __launch_bounds__(256) void k0_ln(const u16* __restrict__ x, u16* __restrict__ xn,
                                             const char* p0, const char* p1) {
  const int tid = threadIdx.x;
  if (blockIdx.x >= 128) {
    int b = blockIdx.x - 128;
    const char* p;
    if (b < 162) p = p0 + (size_t)b * 32768;
    else p = p1 + (size_t)(b - 162) * 32768;
    typedef __attribute__((ext_vector_type(4))) unsigned u32x4;
    u32x4 v[8];
#pragma unroll
    for (int j = 0; j < 8; ++j) v[j] = *(const u32x4*)(p + (size_t)(j * 256 + tid) * 16);
#pragma unroll
    for (int j = 0; j < 8; ++j) asm volatile("" ::"v"(v[j]));
    return;
  }
  const int row = blockIdx.x * 16 + (tid >> 4), l = tid & 15;
  const u16* xr = x + (size_t)row * D_ + l * 4;
  u16x4 v[9];
#pragma unroll
  for (int j = 0; j < 9; ++j) v[j] = *(const u16x4*)(xr + j * 64);
  float sum = 0.f;
#pragma unroll
  for (int j = 0; j < 9; ++j)
#pragma unroll
    for (int e = 0; e < 4; ++e) sum += bf2f(v[j][e]);
  sum += __shfl_xor(sum, 1); sum += __shfl_xor(sum, 2); sum += __shfl_xor(sum, 4); sum += __shfl_xor(sum, 8);
  const float mean = sum * (1.0f / D_);
  float vs = 0.f;
#pragma unroll
  for (int j = 0; j < 9; ++j)
#pragma unroll
    for (int e = 0; e < 4; ++e) { float d = bf2f(v[j][e]) - mean; vs += d * d; }
  vs += __shfl_xor(vs, 1); vs += __shfl_xor(vs, 2); vs += __shfl_xor(vs, 4); vs += __shfl_xor(vs, 8);
  const float mb = rbf(mean);
  const float vb = rbf(vs * (1.0f / D_));
  const float den = rbf(sqrtf(rbf(vb + 1e-5f)));
  const int r = row & 15;
  char* xo = (char*)xn + (size_t)(row >> 4) * 18 * 1024 + r * 64;
#pragma unroll
  for (int j = 0; j < 9; ++j) {
    u16x4 t;
#pragma unroll
    for (int e = 0; e < 4; ++e) t[e] = f2bf(rbf(bf2f(v[j][e]) - mb) / den);
    int col = j * 64 + l * 4;
    int kb = col >> 5, c = (col & 31) >> 3;
    *(u16x4*)(xo + kb * 1024 + ((c ^ swz64(r)) * 16) + (col & 7) * 2) = t;
  }
}

constexpr int K1_XS = 256 * 32 * 2;
constexpr int K1_GS = 32 * 144 * 2;
constexpr int K1_DS = 48 * 32 * 2;
constexpr int K1_SLOT = K1_XS + 2 * K1_GS + K1_DS;
constexpr int K1_LDS = 4 * K1_SLOT + 8192;

template <int NP, int NE, int PB, int EB>
__device__ __forceinline__ void k1_wave(char* smem, const u16* __restrict__ xn, const u16* __restrict__ wg,
                                        const u16* __restrict__ wu, const u16* __restrict__ wdf,
                                        u16* __restrict__ h, u16* __restrict__ a, u16* __restrict__ nb,
                                        const char* sre, const char* sim, const char* wd,
                                        int wave, int lane, int ft, int st) {
  SEGDECL
  const int ws = wave & 3;
  const int f0 = ft * 144, s0 = st * 256;
  const bool hasab = ft < 24;
  const int np = hasab ? 37 : 34;
  const int fr = lane & 15, fq = lane >> 4;
  unsigned voff[5];
#pragma unroll
  for (int j = 0; j < 5; ++j) {
    int i = wave + 8 * j;
    if (i < 16) {
      voff[j] = (unsigned)((s0 / 16 + i) * 18 * 1024 + lane * 16);
    } else if (i >= 34) {
      int r = ((i < 37) ? i - 34 : 0) * 16 + (lane >> 2);
      int c = (lane & 3) ^ swz64(r);
      voff[j] = (unsigned)(r * D_ + c * 8) * 2u;
    } else {
      int jj = (i - 16) % 9;
      int sb = jj * 4 + (lane >> 4), ii = lane & 15;
      int kb = sb / 9, nbk = sb % 9;
      int r = (ii >> 1) ^ ((kb & 1) << 2);
      voff[j] = (unsigned)((kb * 8 + r) * F_ + f0 + nbk * 16 + (ii & 1) * 8) * 2u;
    }
  }
  const bool p5 = wave + 32 < np;
  const unsigned lds0 = __builtin_amdgcn_readfirstlane((unsigned)(size_t)smem);
  auto piece = [&](int kt, int j) {
    int i = wave + 8 * j;
    const u16* base = (i < 16) ? (xn + kt * 512)
                    : (i < 34) ? ((i < 25 ? wg : wu) + (size_t)kt * 32 * F_) : (wdf + kt * 32);
    if (j < 4 || p5) glds16s(base, voff[j], lds0 + (kt & 3) * K1_SLOT + i * 1024);
  };
  auto warm = [&](int n) {
    int kib = blockIdx.x * 85 + n * 8 + wave;
    kib = (n * 8 + wave < 85 && kib < 21568) ? kib : 0;
    const char* base = kib < 8192 ? sre + (size_t)kib * 1024
                     : kib < 16384 ? sim + (size_t)(kib - 8192) * 1024 : wd + (size_t)(kib - 16384) * 1024;
    glds16s(base, lane * 16, lds0 + 4 * K1_SLOT + wave * 1024);
  };
  f32x4 accg[NP][4] = {}, accu[NP][4] = {}, accd[NE][4] = {};
  bf16x8 xf[2][4], df[NE];
  bf16x4 gl[2], gh[2], ul[2], uh[2];
  const int q = (lane & 15) >> 2, p = lane & 3;
  const int sw = (fq & 1) << 2;
  const int xoff = (ws * 64 + fr) * 64 + ((fq ^ swz64(fr)) * 16);
  const int goff0 = K1_XS + (fq * 9 + PB) * 256 + p * 8 + ((q ^ sw) * 32);
  const int goff1 = K1_XS + (fq * 9 + PB) * 256 + p * 8 + (((q + 4) ^ sw) * 32);
  const int doff = K1_XS + 2 * K1_GS + (EB * 16 + fr) * 64 + ((fq ^ swz64(fr)) * 16);
#define K1_RDGU(bi, sb, t, r)                                                   \
  do {                                                                          \
    if ((r) == 0) gl[bi] = tr_read((sb) + goff0 + (t) * 256);                   \
    else if ((r) == 1) gh[bi] = tr_read((sb) + goff1 + (t) * 256);              \
    else if ((r) == 2) ul[bi] = tr_read((sb) + goff0 + (t) * 256 + K1_GS);      \
    else uh[bi] = tr_read((sb) + goff1 + (t) * 256 + K1_GS);                    \
  } while (0)
#define K1_FENCE __builtin_amdgcn_sched_barrier(0)
#define K1_STAGE(SP, KT)                                                        \
  do {                                                                          \
    SEG(4);                                                                     \
    if ((KT) + 2 < 18) {                                                        \
      if ((KT) == 0) { if (p5) WAIT_V(5); else WAIT_V(4); }                     \
      else { if (p5) WAIT_V(6); else WAIT_V(5); }                               \
    } else WAIT_V(0);                                                           \
    SEG(2);                                                                     \
    __syncthreads();                                                            \
    SEG(3);                                                                     \
    const char* cb = smem + ((KT) & 3) * K1_SLOT;                               \
    const char* nx = smem + (((KT) + 1) & 3) * K1_SLOT;                         \
    const bool pf = (KT) + 3 < 18;                                              \
    _Pragma("unroll") for (int t = 0; t < NP; ++t) {                            \
      constexpr int dummy_ = 0; (void)dummy_;                                   \
      const int bc = ((SP) * NP + t) & 1, bn = bc ^ 1;                          \
      _Pragma("unroll") for (int i = 0; i < 8; ++i) {                           \
        const int m = i >> 1;                                                   \
        if ((i & 1) == 0) accg[t][m] = MFMA16(cat2(gl[bc], gh[bc]), xf[SP][m], accg[t][m]); \
        else accu[t][m] = MFMA16(cat2(ul[bc], uh[bc]), xf[SP][m], accu[t][m]);  \
        K1_FENCE;                                                               \
        if (i < 4) {                                                            \
          if (t + 1 < NP) K1_RDGU(bn, cb, t + 1, i); else K1_RDGU(bn, nx, 0, i); \
        } else if (t == NP - 1) {                                               \
          xf[(SP) ^ 1][i - 4] = *(const bf16x8*)(nx + xoff + (i - 4) * 1024);   \
        } else if (t == NP - 2 && i - 4 < NE) {                                 \
          df[i - 4] = *(const bf16x8*)(cb + doff + (i - 4) * 1024);             \
        } else if (i >= 6 && 2 * t + (i - 6) < 5) {                             \
          if (pf) piece((KT) + 3, 2 * t + (i - 6));                             \
        } else if (i == 7 && t == 2) {                                          \
          if (pf) warm(KT);                                                     \
        }                                                                       \
        K1_FENCE;                                                               \
      }                                                                         \
    }                                                                           \
    if (hasab) {                                                                \
      _Pragma("unroll") for (int e = 0; e < NE; ++e)                            \
        _Pragma("unroll") for (int m = 0; m < 4; ++m) accd[e][m] = MFMA16(df[e], xf[SP][m], accd[e][m]); \
    }                                                                           \
  } while (0)
#pragma unroll
  for (int s = 0; s < 3; ++s)
#pragma unroll
    for (int j = 0; j < 5; ++j) piece(s, j);
  SEG(0);
  if (p5) WAIT_V(10); else WAIT_V(8);
  __syncthreads();
  SEG(1);
#pragma unroll
  for (int m = 0; m < 4; ++m) xf[0][m] = *(const bf16x8*)(smem + xoff + m * 1024);
#pragma unroll
  for (int r = 0; r < 4; ++r) K1_RDGU(0, smem, 0, r);
#pragma unroll 1
  for (int kt = 0; kt < 18; kt += 2) {
    K1_STAGE(0, kt);
    K1_STAGE(1, kt + 1);
  }
  SEG(4);
  __syncthreads();
  {
    constexpr int PITCH = NP * 32 + 16;
    char* reg = smem + wave * 11264;
#pragma unroll
    for (int t = 0; t < NP; ++t)
#pragma unroll
      for (int m = 0; m < 4; ++m) {
        u16x4 v;
#pragma unroll
        for (int r = 0; r < 4; ++r) v[r] = f2bf(sigmoidf_(accg[t][m][r]) * accu[t][m][r]);
        *(u16x4*)(reg + (m * 16 + fr) * PITCH + t * 32 + fq * 8) = v;
      }
    constexpr int CPR = NP * 2;
    constexpr int RPI = 64 / CPR;
    const int rr = lane / CPR, cc = lane % CPR;
    const bool act = lane < RPI * CPR;
    u16* hrow = h + (size_t)(s0 + ws * 64) * F_ + f0 + PB * 16 + cc * 8;
#pragma unroll
    for (int i = 0; i < (64 + RPI - 1) / RPI; ++i) {
      int row = i * RPI + rr;
      if (act && row < 64) {
        u16x8 v = *(const u16x8*)(reg + row * PITCH + cc * 16);
        *(u16x8*)(hrow + (size_t)row * F_) = v;
      }
    }
  }
  if (hasab) {
    const bool isim = ft >= 12;
    const float sg = isim ? -1.f : 1.f;
#pragma unroll
    for (int e = 0; e < NE; ++e) {
      const int ec = (isim ? ft - 12 : ft) * 48 + (EB + e) * 16;
      const int nt = ec / 144, nbk = (ec % 144) >> 4;
      char* o = (char*)(isim ? nb : a) + (size_t)nt * 32 * 18432 + nbk * 256 + fq * 8;
#pragma unroll
      for (int m = 0; m < 4; ++m) {
        u16x4 v;
#pragma unroll
        for (int r = 0; r < 4; ++r) v[r] = f2bf(sg * accd[e][m][r]);
        int s = s0 + ws * 64 + m * 16 + fr;
        int stg = s >> 6, kb = (s & 63) >> 3, rr = (s & 7) ^ ((kb & 1) << 2);
        *(u16x4*)(o + (size_t)stg * 18432 + kb * 2304 + rr * 32) = v;
      }
    }
  }
}

__global__ __launch_bounds__(512) void k1_glu(const u16* __restrict__ xn, const u16* __restrict__ wg,
                                              const u16* __restrict__ wu, const u16* __restrict__ wre,
                                              const u16* __restrict__ wim, u16* __restrict__ h,
                                              u16* __restrict__ a, u16* __restrict__ nb,
                                              const char* sre, const char* sim, const char* wd) {
  extern __shared__ __attribute__((aligned(16))) char smem[];
  const int tid = threadIdx.x, lane = tid & 63, wave = __builtin_amdgcn_readfirstlane(tid >> 6);
  const int xcd = blockIdx.x & 7, idx = blockIdx.x >> 3;
  const int ft = xcd * 4 + (idx >> 3), st = idx & 7;
  const u16* wdf = (ft < 12 ? wre + (size_t)ft * 48 * D_ : wim + (size_t)(ft < 24 ? ft - 12 : 0) * 48 * D_);
  if (wave < 4) k1_wave<5, 1, 0, 0>(smem, xn, wg, wu, wdf, h, a, nb, sre, sim, wd, wave, lane, ft, st);
  else {
    __builtin_amdgcn_s_setprio(1);
    k1_wave<4, 2, 5, 1>(smem, xn, wg, wu, wdf, h, a, nb, sre, sim, wd, wave, lane, ft, st);
  }
}

constexpr int K2_AS = 128 * 64 * 2;
constexpr int K2_BS = 64 * 144 * 2;
constexpr int K2_SLOT = K2_AS + K2_BS;
constexpr int K2_LDS = 4 * K2_SLOT;
__global__ __launch_bounds__(512) void k2_gemm(const u16* __restrict__ sre, const u16* __restrict__ sim,
                                               const u16* __restrict__ a, const u16* __restrict__ nb,
                                               const u16* __restrict__ h, const u16* __restrict__ wd,
                                               u16* __restrict__ yre, u16* __restrict__ ynim,
                                               float* __restrict__ olo, float* __restrict__ ohi) {
  extern __shared__ __attribute__((aligned(16))) char smem[];
  const int tid = threadIdx.x, lane = tid & 63, wave = __builtin_amdgcn_readfirstlane(tid >> 6);
  const int xcd = blockIdx.x & 7, idx = blockIdx.x >> 3;
  const int sp = xcd >> 1;
  const int mt = (xcd & 1) * 8 + (idx >> 2), nt = idx & 3;
  const int t0 = mt * 128, e0 = nt * 144;
  const int nst = sp < 2 ? 32 : 36;
  if (wave >= 4) {
    const int lw = wave - 4;
    const u16* A; const u16* B; int lda; int bstep;
    const bool img = sp < 2;
    if (sp == 0) { A = sre; B = a + (size_t)nt * 32 * 9216; lda = S_; bstep = 9216; }
    else if (sp == 1) { A = sim; B = nb + (size_t)nt * 32 * 9216; lda = S_; bstep = 9216; }
    else if (sp == 2) { A = h; B = wd; lda = F_; bstep = 64 * D_; }
    else { A = h + 2304; B = wd + (size_t)2304 * D_; lda = F_; bstep = 64 * D_; }
    unsigned voff[9];
#pragma unroll
    for (int j = 0; j < 9; ++j) {
      int i = lw + 4 * j;
      if (i < 16) {
        int r = i * 8 + (lane >> 3);
        int c = (lane & 7) ^ (lane >> 3);
        voff[j] = (unsigned)((t0 + r) * lda + c * 8) * 2u;
      } else {
        int jj = (i < 34) ? (i - 16) : 0;
        if (img) {
          voff[j] = (unsigned)(jj * 1024 + lane * 16);
        } else {
          int sb = jj * 4 + (lane >> 4), ii = lane & 15;
          int kb = sb / 9, nbk = sb % 9;
          int r = (ii >> 1) ^ ((kb & 1) << 2);
          voff[j] = (unsigned)((kb * 8 + r) * D_ + e0 + nbk * 16 + (ii & 1) * 8) * 2u;
        }
      }
    }
    const bool p9 = lw < 2;
    const unsigned lds0 = __builtin_amdgcn_readfirstlane((unsigned)(size_t)smem);
    auto stage = [&](int kt) {
#pragma unroll
      for (int j = 0; j < 9; ++j) {
        const u16* base = (j < 4) ? (A + kt * 64) : (B + (size_t)kt * bstep);
        if (j < 8 || p9) glds16s(base, voff[j], lds0 + (kt & 3) * K2_SLOT + (lw + 4 * j) * 1024);
      }
    };
    stage(0); stage(1);
    if (p9) WAIT_V(9); else WAIT_V(8);
    __syncthreads();
    stage(2); stage(3);
#pragma unroll 1
    for (int kt = 0; kt < nst; ++kt) {
      const int rem = nst - 2 - kt;
      if (rem >= 2) { if (p9) WAIT_V(18); else WAIT_V(16); }
      else if (rem == 1) { if (p9) WAIT_V(9); else WAIT_V(8); }
      else WAIT_V(0);
      __syncthreads();
      if (kt + 4 < nst) stage(kt + 4);
    }
    __syncthreads();
    __syncthreads();
    return;
  }
  const int wt = wave & 1, wk = wave >> 1;
  const int fr = lane & 15, fq = lane >> 4;
  const int q = (lane & 15) >> 2, p = lane & 3;
  const int sw = (fq & 1) << 2;
  f32x4 acc[9][4] = {};
  bf16x8 afA[4], afB[4];
  bf16x4 blA[9], bhA[9], blB[9], bhB[9];
  const int aoff = (wt * 64 + fr) * 128 + (((wk * 4 + fq) ^ (fr & 7)) * 16);
  const int boff0 = K2_AS + (wk * 4 + fq) * 9 * 256 + p * 8 + ((q ^ sw) * 32);
  const int boff1 = K2_AS + (wk * 4 + fq) * 9 * 256 + p * 8 + (((q + 4) ^ sw) * 32);
#define RD(AFN, BLN, BHN, r, base)                                              \
  do {                                                                          \
    if ((r) < 4) AFN[(r) & 3] = *(const bf16x8*)((base) + aoff + ((r) & 3) * 2048); \
    else if ((((r) - 4) & 1) == 0) BLN[((r) - 4) >> 1] = tr_read((base) + boff0 + (((r) - 4) >> 1) * 256); \
    else BHN[((r) - 4) >> 1] = tr_read((base) + boff1 + (((r) - 4) >> 1) * 256); \
  } while (0)
#define ITER(AFC, BLC, BHC, AFN, BLN, BHN, KT)                                  \
  do {                                                                          \
    __syncthreads();                                                            \
    const char* nb_ = smem + (((KT) + 1) & 3) * K2_SLOT;                        \
    _Pragma("unroll") for (int i = 0; i < 36; ++i) {                            \
      acc[i >> 2][i & 3] = MFMA16(cat2(BLC[i >> 2], BHC[i >> 2]), AFC[i & 3], acc[i >> 2][i & 3]); \
      __builtin_amdgcn_sched_barrier(0);                                        \
      if (i < 22) { RD(AFN, BLN, BHN, i, nb_); __builtin_amdgcn_sched_barrier(0); } \
    }                                                                           \
  } while (0)
  __syncthreads();
#pragma unroll
  for (int r = 0; r < 22; ++r) RD(afA, blA, bhA, r, smem);
#pragma unroll 1
  for (int kt = 0; kt < nst; kt += 2) {
    ITER(afA, blA, bhA, afB, blB, bhB, kt);
    ITER(afB, blB, bhB, afA, blA, bhA, kt + 1);
  }
  __syncthreads();
  {
    char* dst = smem + wave * 20480 + lane * 16;
#pragma unroll
    for (int n = 0; n < 9; ++n)
#pragma unroll
      for (int m = 0; m < 4; ++m) {
        bool mine = (wk == 0) ? (n < 5) : (n >= 5);
        int slot = (wk == 0) ? (n - 5) : n;
        if (!mine) *(f32x4*)(dst + (slot * 4 + m) * 1024) = acc[n][m];
      }
  }
  __syncthreads();
  {
    const char* s = smem + (wave ^ 2) * 20480 + lane * 16;
#pragma unroll
    for (int n = 0; n < 9; ++n)
#pragma unroll
      for (int m = 0; m < 4; ++m) {
        bool mine = (wk == 0) ? (n < 5) : (n >= 5);
        int slot = (wk == 0) ? n : (n - 5);
        if (mine) {
          f32x4 v = acc[n][m] + *(const f32x4*)(s + (slot * 4 + m) * 1024);
          size_t off = (size_t)(t0 + wt * 64 + m * 16 + fr) * D_ + e0 + n * 16 + fq * 4;
          if (sp < 2) {
            u16x4 w;
#pragma unroll
            for (int r = 0; r < 4; ++r) w[r] = f2bf(v[r]);
            *(u16x4*)((sp == 0 ? yre : ynim) + off) = w;
          } else {
            *(f32x4*)((sp == 2 ? olo : ohi) + off) = v;
          }
        }
      }
  }
}

__global__ __launch_bounds__(256) void k3_combine(const u16* __restrict__ x, const u16* __restrict__ yre,
                                                  const u16* __restrict__ ynim, const float* __restrict__ olo,
                                                  const float* __restrict__ ohi, u16* __restrict__ out) {
  const size_t i = ((size_t)blockIdx.x * 256 + threadIdx.x) * 8;
  u16x8 xv = *(const u16x8*)(x + i), a = *(const u16x8*)(yre + i), b = *(const u16x8*)(ynim + i);
  f32x4 l0 = *(const f32x4*)(olo + i), l1 = *(const f32x4*)(olo + i + 4);
  f32x4 h0 = *(const f32x4*)(ohi + i), h1 = *(const f32x4*)(ohi + i + 4);
  u16x8 r;
#pragma unroll
  for (int j = 0; j < 8; ++j) {
    float s = rbf(bf2f(a[j]) + bf2f(b[j]));
    float o = rbf((j < 4 ? l0[j] : l1[j - 4]) + (j < 4 ? h0[j] : h1[j - 4]));
    float g = rbf(o * sigmoidf_(o));
    r[j] = f2bf(bf2f(xv[j]) + rbf(s * g));
  }
  *(u16x8*)(out + i) = r;
}

extern "C" void kernel_launch(void* const* d_in, const int* in_sizes, int n_in,
                              void* d_out, int out_size, void* d_ws, size_t ws_size,
                              hipStream_t stream) {
  const u16* x = (const u16*)d_in[0];
  const u16* sre = (const u16*)d_in[1];
  const u16* sim = (const u16*)d_in[2];
  const u16* wre = (const u16*)d_in[3];
  const u16* wim = (const u16*)d_in[4];
  const u16* wg = (const u16*)d_in[6];
  const u16* wu = (const u16*)d_in[7];
  const u16* wd = (const u16*)d_in[8];
  char* ws = (char*)d_ws;
  const size_t SD2 = (size_t)S_ * D_ * 2;
  u16* xn = (u16*)(ws);
  u16* a = (u16*)(ws + SD2);
  u16* nb = (u16*)(ws + 2 * SD2);
  u16* yre = (u16*)(ws + 3 * SD2);
  u16* ynim = (u16*)(ws + 4 * SD2);
  float* olo = (float*)(ws + 5 * SD2);
  float* ohi = (float*)(ws + 7 * SD2);
  u16* h = (u16*)(ws + 9 * SD2);
  (void)hipFuncSetAttribute((const void*)k1_glu, hipFuncAttributeMaxDynamicSharedMemorySize, K1_LDS);
  (void)hipFuncSetAttribute((const void*)k2_gemm, hipFuncAttributeMaxDynamicSharedMemorySize, K2_LDS);
  k0_ln<<<128 + 324, 256, 0, stream>>>(x, xn, (const char*)wg, (const char*)wu);
  k1_glu<<<256, 512, K1_LDS, stream>>>(xn, wg, wu, wre, wim, h, a, nb, (const char*)sre, (const char*)sim, (const char*)wd);
  k2_gemm<<<256, 512, K2_LDS, stream>>>(sre, sim, a, nb, h, wd, yre, ynim, olo, ohi);
  k3_combine<<<(S_ * D_ / 8) / 256, 256, 0, stream>>>(x, yre, ynim, olo, ohi, (u16*)d_out);
}
```

```cpp
#include <hip/hip_runtime.h>
#include <hip/hip_bf16.h>

typedef unsigned short u16;
typedef __attribute__((ext_vector_type(8))) short bf16x8;
typedef __attribute__((ext_vector_type(4))) short bf16x4;
typedef __attribute__((ext_vector_type(4))) float f32x4;
typedef __attribute__((ext_vector_type(4))) unsigned short u16x4;
typedef __attribute__((ext_vector_type(8))) unsigned short u16x8;
typedef __attribute__((address_space(3))) bf16x4 lds_bf16x4;

constexpr int S_ = 2048, D_ = 576, F_ = 4608;

__device__ __forceinline__ float bf2f(u16 v) { return __uint_as_float(((unsigned)v) << 16); }
__device__ __forceinline__ u16 f2bf(float f) { __bf16 b = (__bf16)f; return __builtin_bit_cast(u16, b); }
__device__ __forceinline__ float rbf(float f) { return bf2f(f2bf(f)); }
__device__ __forceinline__ float sigmoidf_(float x) {
  return __builtin_amdgcn_rcpf(1.0f + __builtin_amdgcn_exp2f(-1.4426950408889634f * x));
}

__device__ __forceinline__ void glds16s(const void* sbase, unsigned voff, unsigned lds_dst) {
  unsigned keep;
  asm volatile("s_mov_b32 %0, m0\n\ts_mov_b32 m0, %3\n\ts_nop 0\n\tglobal_load_lds_dwordx4 %1, %2\n\ts_mov_b32 m0, %0"
               : "=&s"(keep) : "v"(voff), "s"(sbase), "s"(lds_dst) : "memory");
}
__device__ __forceinline__ bf16x4 tr_read(const char* p) {
  return __builtin_amdgcn_ds_read_tr16_b64_v4i16((lds_bf16x4*)p);
}
__device__ __forceinline__ bf16x8 cat2(bf16x4 lo, bf16x4 hi) {
  return __builtin_shufflevector(lo, hi, 0, 1, 2, 3, 4, 5, 6, 7);
}
#define MFMA16(a, b, c) __builtin_amdgcn_mfma_f32_16x16x32_bf16((a), (b), (c), 0, 0, 0)
#define WAIT_V(n) asm volatile("s_waitcnt vmcnt(" #n ")" ::: "memory")
__device__ __forceinline__ int swz64(int row) { return (-(row >> 2)) & 3; }

__global__ __launch_bounds__(256) void k0_ln(const u16* __restrict__ x, u16* __restrict__ xn,
                                             const char* p0, const char* p1) {
  const int tid = threadIdx.x;
  if (blockIdx.x >= 128) {
    int b = blockIdx.x - 128;
    const char* p;
    if (b < 162) p = p0 + (size_t)b * 32768;
    else p = p1 + (size_t)(b - 162) * 32768;
    typedef __attribute__((ext_vector_type(4))) unsigned u32x4;
    u32x4 v[8];
#pragma unroll
    for (int j = 0; j < 8; ++j) v[j] = *(const u32x4*)(p + (size_t)(j * 256 + tid) * 16);
#pragma unroll
    for (int j = 0; j < 8; ++j) asm volatile("" ::"v"(v[j]));
    return;
  }
  const int row = blockIdx.x * 16 + (tid >> 4), l = tid & 15;
  const u16* xr = x + (size_t)row * D_ + l * 4;
  u16x4 v[9];
#pragma unroll
  for (int j = 0; j < 9; ++j) v[j] = *(const u16x4*)(xr + j * 64);
  float sum = 0.f;
#pragma unroll
  for (int j = 0; j < 9; ++j)
#pragma unroll
    for (int e = 0; e < 4; ++e) sum += bf2f(v[j][e]);
  sum += __shfl_xor(sum, 1); sum += __shfl_xor(sum, 2); sum += __shfl_xor(sum, 4); sum += __shfl_xor(sum, 8);
  const float mean = sum * (1.0f / D_);
  float vs = 0.f;
#pragma unroll
  for (int j = 0; j < 9; ++j)
#pragma unroll
    for (int e = 0; e < 4; ++e) { float d = bf2f(v[j][e]) - mean; vs += d * d; }
  vs += __shfl_xor(vs, 1); vs += __shfl_xor(vs, 2); vs += __shfl_xor(vs, 4); vs += __shfl_xor(vs, 8);
  const float mb = rbf(mean);
  const float vb = rbf(vs * (1.0f / D_));
  const float den = rbf(sqrtf(rbf(vb + 1e-5f)));
  const int r = row & 15;
  char* xo = (char*)xn + (size_t)(row >> 4) * 18 * 1024 + r * 64;
#pragma unroll
  for (int j = 0; j < 9; ++j) {
    u16x4 t;
#pragma unroll
    for (int e = 0; e < 4; ++e) t[e] = f2bf(rbf(bf2f(v[j][e]) - mb) / den);
    int col = j * 64 + l * 4;
    int kb = col >> 5, c = (col & 31) >> 3;
    *(u16x4*)(xo + kb * 1024 + ((c ^ swz64(r)) * 16) + (col & 7) * 2) = t;
  }
}

constexpr int K1_XS = 256 * 32 * 2;
constexpr int K1_GS = 32 * 144 * 2;
constexpr int K1_DS = 48 * 32 * 2;
constexpr int K1_SLOT = K1_XS + 2 * K1_GS + K1_DS;
constexpr int K1_LDS = 4 * K1_SLOT + 8192;

template <int NP, int NE, int PB, int EB>
__device__ __forceinline__ void k1_wave(char* smem, const u16* __restrict__ xn, const u16* __restrict__ wg,
                                        const u16* __restrict__ wu, const u16* __restrict__ wdf,
                                        u16* __restrict__ h, u16* __restrict__ a, u16* __restrict__ nb,
                                        const char* sre, const char* sim, const char* wd,
                                        int wave, int lane, int ft, int st) {
  const int ws = wave & 3;
  const int f0 = ft * 144, s0 = st * 256;
  const bool hasab = ft < 24;
  const int np = hasab ? 37 : 34;
  const int fr = lane & 15, fq = lane >> 4;
  unsigned voff[5];
#pragma unroll
  for (int j = 0; j < 5; ++j) {
    int i = wave + 8 * j;
    if (i < 16) {
      voff[j] = (unsigned)((s0 / 16 + i) * 18 * 1024 + lane * 16);
    } else if (i >= 34) {
      int r = ((i < 37) ? i - 34 : 0) * 16 + (lane >> 2);
      int c = (lane & 3) ^ swz64(r);
      voff[j] = (unsigned)(r * D_ + c * 8) * 2u;
    } else {
      int jj = (i - 16) % 9;
      int sb = jj * 4 + (lane >> 4), ii = lane & 15;
      int kb = sb / 9, nbk = sb % 9;
      int r = (ii >> 1) ^ ((kb & 1) << 2);
      voff[j] = (unsigned)((kb * 8 + r) * F_ + f0 + nbk * 16 + (ii & 1) * 8) * 2u;
    }
  }
  const bool p5 = wave + 32 < np;
  const unsigned lds0 = __builtin_amdgcn_readfirstlane((unsigned)(size_t)smem);
  auto piece = [&](int kt, int j) {
    int i = wave + 8 * j;
    const u16* base = (i < 16) ? (xn + kt * 512)
                    : (i < 34) ? ((i < 25 ? wg : wu) + (size_t)kt * 32 * F_) : (wdf + kt * 32);
    if (j < 4 || p5) glds16s(base, voff[j], lds0 + (kt & 3) * K1_SLOT + i * 1024);
  };
  auto warm = [&](int n) {
    int kib = blockIdx.x * 85 + n * 8 + wave;
    kib = (n * 8 + wave < 85 && kib < 21568) ? kib : 0;
    const char* base = kib < 8192 ? sre + (size_t)kib * 1024
                     : kib < 16384 ? sim + (size_t)(kib - 8192) * 1024 : wd + (size_t)(kib - 16384) * 1024;
    glds16s(base, lane * 16, lds0 + 4 * K1_SLOT + wave * 1024);
  };
  f32x4 accg[NP][4] = {}, accu[NP][4] = {}, accd[NE][4] = {};
  bf16x8 xf[2][4], df[NE];
  bf16x4 gl[2], gh[2], ul[2], uh[2];
  const int q = (lane & 15) >> 2, p = lane & 3;
  const int sw = (fq & 1) << 2;
  const int xoff = (ws * 64 + fr) * 64 + ((fq ^ swz64(fr)) * 16);
  const int goff0 = K1_XS + (fq * 9 + PB) * 256 + p * 8 + ((q ^ sw) * 32);
  const int goff1 = K1_XS + (fq * 9 + PB) * 256 + p * 8 + (((q + 4) ^ sw) * 32);
  const int doff = K1_XS + 2 * K1_GS + (EB * 16 + fr) * 64 + ((fq ^ swz64(fr)) * 16);
#define K1_RDGU(bi, sb, t, r)                                                   \
  do {                                                                          \
    if ((r) == 0) gl[bi] = tr_read((sb) + goff0 + (t) * 256);                   \
    else if ((r) == 1) gh[bi] = tr_read((sb) + goff1 + (t) * 256);              \
    else if ((r) == 2) ul[bi] = tr_read((sb) + goff0 + (t) * 256 + K1_GS);      \
    else uh[bi] = tr_read((sb) + goff1 + (t) * 256 + K1_GS);                    \
  } while (0)
#define K1_FENCE __builtin_amdgcn_sched_barrier(0)
#define K1_STAGE(SP, KT)                                                        \
  do {                                                                          \
    if ((KT) + 2 < 18) {                                                        \
      if ((KT) == 0) { if (p5) WAIT_V(5); else WAIT_V(4); }                     \
      else { if (p5) WAIT_V(6); else WAIT_V(5); }                               \
    } else WAIT_V(0);                                                           \
    __syncthreads();                                                            \
    const char* cb = smem + ((KT) & 3) * K1_SLOT;                               \
    const char* nx = smem + (((KT) + 1) & 3) * K1_SLOT;                         \
    const bool pf = (KT) + 3 < 18;                                              \
    _Pragma("unroll") for (int t = 0; t < NP; ++t) {                            \
      constexpr int dummy_ = 0; (void)dummy_;                                   \
      const int bc = ((SP) * NP + t) & 1, bn = bc ^ 1;                          \
      _Pragma("unroll") for (int i = 0; i < 8; ++i) {                           \
        const int m = i >> 1;                                                   \
        if ((i & 1) == 0) accg[t][m] = MFMA16(cat2(gl[bc], gh[bc]), xf[SP][m], accg[t][m]); \
        else accu[t][m] = MFMA16(cat2(ul[bc], uh[bc]), xf[SP][m], accu[t][m]);  \
        K1_FENCE;                                                               \
        if (i < 4) {                                                            \
          if (t + 1 < NP) K1_RDGU(bn, cb, t + 1, i); else K1_RDGU(bn, nx, 0, i); \
        } else if (t == NP - 1) {                                               \
          xf[(SP) ^ 1][i - 4] = *(const bf16x8*)(nx + xoff + (i - 4) * 1024);   \
        } else if (t == NP - 2 && i - 4 < NE) {                                 \
          df[i - 4] = *(const bf16x8*)(cb + doff + (i - 4) * 1024);             \
        } else if (i >= 6 && 2 * t + (i - 6) < 5) {                             \
          if (pf) piece((KT) + 3, 2 * t + (i - 6));                             \
        } else if (i == 7 && t == 2) {                                          \
          if (pf) warm(KT);                                                     \
        }                                                                       \
        K1_FENCE;                                                               \
      }                                                                         \
    }                                                                           \
    if (hasab) {                                                                \
      _Pragma("unroll") for (int e = 0; e < NE; ++e)                            \
        _Pragma("unroll") for (int m = 0; m < 4; ++m) accd[e][m] = MFMA16(df[e], xf[SP][m], accd[e][m]); \
    }                                                                           \
  } while (0)
#pragma unroll
  for (int s = 0; s < 3; ++s)
#pragma unroll
    for (int j = 0; j < 5; ++j) piece(s, j);
  if (p5) WAIT_V(10); else WAIT_V(8);
  __syncthreads();
#pragma unroll
  for (int m = 0; m < 4; ++m) xf[0][m] = *(const bf16x8*)(smem + xoff + m * 1024);
#pragma unroll
  for (int r = 0; r < 4; ++r) K1_RDGU(0, smem, 0, r);
#pragma unroll 1
  for (int kt = 0; kt < 18; kt += 2) {
    K1_STAGE(0, kt);
    K1_STAGE(1, kt + 1);
  }
  __syncthreads();
  {
    constexpr int PITCH = NP * 32 + 16;
    char* reg = smem + wave * 11264;
#pragma unroll
    for (int t = 0; t < NP; ++t)
#pragma unroll
      for (int m = 0; m < 4; ++m) {
        u16x4 v;
#pragma unroll
        for (int r = 0; r < 4; ++r) v[r] = f2bf(sigmoidf_(accg[t][m][r]) * accu[t][m][r]);
        *(u16x4*)(reg + (m * 16 + fr) * PITCH + t * 32 + fq * 8) = v;
      }
    constexpr int CPR = NP * 2;
    constexpr int RPI = 64 / CPR;
    const int rr = lane / CPR, cc = lane % CPR;
    const bool act = lane < RPI * CPR;
    u16* hrow = h + (size_t)(s0 + ws * 64) * F_ + f0 + PB * 16 + cc * 8;
#pragma unroll
    for (int i = 0; i < (64 + RPI - 1) / RPI; ++i) {
      int row = i * RPI + rr;
      if (act && row < 64) {
        u16x8 v = *(const u16x8*)(reg + row * PITCH + cc * 16);
        *(u16x8*)(hrow + (size_t)row * F_) = v;
      }
    }
  }
  if (hasab) {
    const bool isim = ft >= 12;
    const float sg = isim ? -1.f : 1.f;
#pragma unroll
    for (int e = 0; e < NE; ++e) {
      const int ec = (isim ? ft - 12 : ft) * 48 + (EB + e) * 16;
      const int nt = ec / 144, nbk = (ec % 144) >> 4;
      char* o = (char*)(isim ? nb : a) + (size_t)nt * 32 * 18432 + nbk * 256 + fq * 8;
#pragma unroll
      for (int m = 0; m < 4; ++m) {
        u16x4 v;
#pragma unroll
        for (int r = 0; r < 4; ++r) v[r] = f2bf(sg * accd[e][m][r]);
        int s = s0 + ws * 64 + m * 16 + fr;
        int stg = s >> 6, kb = (s & 63) >> 3, rr = (s & 7) ^ ((kb & 1) << 2);
        *(u16x4*)(o + (size_t)stg * 18432 + kb * 2304 + rr * 32) = v;
      }
    }
  }
}

__global__ __launch_bounds__(512) void k1_glu(const u16* __restrict__ xn, const u16* __restrict__ wg,
                                              const u16* __restrict__ wu, const u16* __restrict__ wre,
                                              const u16* __restrict__ wim, u16* __restrict__ h,
                                              u16* __restrict__ a, u16* __restrict__ nb,
                                              const char* sre, const char* sim, const char* wd) {
  extern __shared__ __attribute__((aligned(16))) char smem[];
  const int tid = threadIdx.x, lane = tid & 63, wave = __builtin_amdgcn_readfirstlane(tid >> 6);
  const int xcd = blockIdx.x & 7, idx = blockIdx.x >> 3;
  const int ft = xcd * 4 + (idx >> 3), st = idx & 7;
  const u16* wdf = (ft < 12 ? wre + (size_t)ft * 48 * D_ : wim + (size_t)(ft < 24 ? ft - 12 : 0) * 48 * D_);
  if (wave < 4) k1_wave<5, 1, 0, 0>(smem, xn, wg, wu, wdf, h, a, nb, sre, sim, wd, wave, lane, ft, st);
  else {
    __builtin_amdgcn_s_setprio(1);
    k1_wave<4, 2, 5, 1>(smem, xn, wg, wu, wdf, h, a, nb, sre, sim, wd, wave, lane, ft, st);
  }
}

constexpr int K2_AS = 128 * 64 * 2;
constexpr int K2_BS = 64 * 144 * 2;
constexpr int K2_SLOT = K2_AS + K2_BS;
constexpr int K2_LDS = 4 * K2_SLOT;
__global__ __launch_bounds__(512) void k2_gemm(const u16* __restrict__ sre, const u16* __restrict__ sim,
                                               const u16* __restrict__ a, const u16* __restrict__ nb,
                                               const u16* __restrict__ h, const u16* __restrict__ wd,
                                               u16* __restrict__ yre, u16* __restrict__ ynim,
                                               float* __restrict__ olo, float* __restrict__ ohi) {
  extern __shared__ __attribute__((aligned(16))) char smem[];
  const int tid = threadIdx.x, lane = tid & 63, wave = __builtin_amdgcn_readfirstlane(tid >> 6);
  const int xcd = blockIdx.x & 7, idx = blockIdx.x >> 3;
  const int sp = xcd >> 1;
  const int mt = (xcd & 1) * 8 + (idx >> 2), nt = idx & 3;
  const int t0 = mt * 128, e0 = nt * 144;
  const int nst = sp < 2 ? 32 : 36;
  if (wave >= 4) {
    const int lw = wave - 4;
    const u16* A; const u16* B; int lda; int bstep;
    const bool img = sp < 2;
    if (sp == 0) { A = sre; B = a + (size_t)nt * 32 * 9216; lda = S_; bstep = 9216; }
    else if (sp == 1) { A = sim; B = nb + (size_t)nt * 32 * 9216; lda = S_; bstep = 9216; }
    else if (sp == 2) { A = h; B = wd; lda = F_; bstep = 64 * D_; }
    else { A = h + 2304; B = wd + (size_t)2304 * D_; lda = F_; bstep = 64 * D_; }
    unsigned voff[9];
#pragma unroll
    for (int j = 0; j < 9; ++j) {
      int i = lw + 4 * j;
      if (i < 16) {
        int r = i * 8 + (lane >> 3);
        int c = (lane & 7) ^ (lane >> 3);
        voff[j] = (unsigned)((t0 + r) * lda + c * 8) * 2u;
      } else {
        int jj = (i < 34) ? (i - 16) : 0;
        if (img) {
          voff[j] = (unsigned)(jj * 1024 + lane * 16);
        } else {
          int sb = jj * 4 + (lane >> 4), ii = lane & 15;
          int kb = sb / 9, nbk = sb % 9;
          int r = (ii >> 1) ^ ((kb & 1) << 2);
          voff[j] = (unsigned)((kb * 8 + r) * D_ + e0 + nbk * 16 + (ii & 1) * 8) * 2u;
        }
      }
    }
    const bool p9 = lw < 2;
    const unsigned lds0 = __builtin_amdgcn_readfirstlane((unsigned)(size_t)smem);
    auto stage = [&](int kt) {
#pragma unroll
      for (int j = 0; j < 9; ++j) {
        const u16* base = (j < 4) ? (A + kt * 64) : (B + (size_t)kt * bstep);
        if (j < 8 || p9) glds16s(base, voff[j], lds0 + (kt & 3) * K2_SLOT + (lw + 4 * j) * 1024);
      }
    };
    stage(0); stage(1);
    if (p9) WAIT_V(9); else WAIT_V(8);
    __syncthreads();
    stage(2); stage(3);
#pragma unroll 1
    for (int kt = 0; kt < nst; ++kt) {
      const int rem = nst - 2 - kt;
      if (rem >= 2) { if (p9) WAIT_V(18); else WAIT_V(16); }
      else if (rem == 1) { if (p9) WAIT_V(9); else WAIT_V(8); }
      else WAIT_V(0);
      __syncthreads();
      if (kt + 4 < nst) stage(kt + 4);
    }
    __syncthreads();
    __syncthreads();
    return;
  }
  const int wt = wave & 1, wk = wave >> 1;
  const int fr = lane & 15, fq = lane >> 4;
  const int q = (lane & 15) >> 2, p = lane & 3;
  const int sw = (fq & 1) << 2;
  f32x4 acc[9][4] = {};
  bf16x8 afA[4], afB[4];
  bf16x4 blA[9], bhA[9], blB[9], bhB[9];
  const int aoff = (wt * 64 + fr) * 128 + (((wk * 4 + fq) ^ (fr & 7)) * 16);
  const int boff0 = K2_AS + (wk * 4 + fq) * 9 * 256 + p * 8 + ((q ^ sw) * 32);
  const int boff1 = K2_AS + (wk * 4 + fq) * 9 * 256 + p * 8 + (((q + 4) ^ sw) * 32);
#define RD(AFN, BLN, BHN, r, base)                                              \
  do {                                                                          \
    if ((r) < 4) AFN[(r) & 3] = *(const bf16x8*)((base) + aoff + ((r) & 3) * 2048); \
    else if ((((r) - 4) & 1) == 0) BLN[((r) - 4) >> 1] = tr_read((base) + boff0 + (((r) - 4) >> 1) * 256); \
    else BHN[((r) - 4) >> 1] = tr_read((base) + boff1 + (((r) - 4) >> 1) * 256); \
  } while (0)
#define ITER(AFC, BLC, BHC, AFN, BLN, BHN, KT)                                  \
  do {                                                                          \
    __syncthreads();                                                            \
    const char* nb_ = smem + (((KT) + 1) & 3) * K2_SLOT;                        \
    _Pragma("unroll") for (int i = 0; i < 36; ++i) {                            \
      acc[i >> 2][i & 3] = MFMA16(cat2(BLC[i >> 2], BHC[i >> 2]), AFC[i & 3], acc[i >> 2][i & 3]); \
      __builtin_amdgcn_sched_barrier(0);                                        \
      if (i < 22) { RD(AFN, BLN, BHN, i, nb_); __builtin_amdgcn_sched_barrier(0); } \
    }                                                                           \
  } while (0)
  __syncthreads();
#pragma unroll
  for (int r = 0; r < 22; ++r) RD(afA, blA, bhA, r, smem);
#pragma unroll 1
  for (int kt = 0; kt < nst; kt += 2) {
    ITER(afA, blA, bhA, afB, blB, bhB, kt);
    ITER(afB, blB, bhB, afA, blA, bhA, kt + 1);
  }
  __syncthreads();
  {
    char* dst = smem + wave * 20480 + lane * 16;
#pragma unroll
    for (int n = 0; n < 9; ++n)
#pragma unroll
      for (int m = 0; m < 4; ++m) {
        bool mine = (wk == 0) ? (n < 5) : (n >= 5);
        int slot = (wk == 0) ? (n - 5) : n;
        if (!mine) *(f32x4*)(dst + (slot * 4 + m) * 1024) = acc[n][m];
      }
  }
  __syncthreads();
  {
    const char* s = smem + (wave ^ 2) * 20480 + lane * 16;
#pragma unroll
    for (int n = 0; n < 9; ++n)
#pragma unroll
      for (int m = 0; m < 4; ++m) {
        bool mine = (wk == 0) ? (n < 5) : (n >= 5);
        int slot = (wk == 0) ? n : (n - 5);
        if (mine) {
          f32x4 v = acc[n][m] + *(const f32x4*)(s + (slot * 4 + m) * 1024);
          size_t off = (size_t)(t0 + wt * 64 + m * 16 + fr) * D_ + e0 + n * 16 + fq * 4;
          if (sp < 2) {
            u16x4 w;
#pragma unroll
            for (int r = 0; r < 4; ++r) w[r] = f2bf(v[r]);
            *(u16x4*)((sp == 0 ? yre : ynim) + off) = w;
          } else {
            *(f32x4*)((sp == 2 ? olo : ohi) + off) = v;
          }
        }
      }
  }
}

__global__ __launch_bounds__(256) void k3_combine(const u16* __restrict__ x, const u16* __restrict__ yre,
                                                  const u16* __restrict__ ynim, const float* __restrict__ olo,
                                                  const float* __restrict__ ohi, u16* __restrict__ out) {
  const size_t i = ((size_t)blockIdx.x * 256 + threadIdx.x) * 8;
  u16x8 xv = *(const u16x8*)(x + i), a = *(const u16x8*)(yre + i), b = *(const u16x8*)(ynim + i);
  f32x4 l0 = *(const f32x4*)(olo + i), l1 = *(const f32x4*)(olo + i + 4);
  f32x4 h0 = *(const f32x4*)(ohi + i), h1 = *(const f32x4*)(ohi + i + 4);
  u16x8 r;
#pragma unroll
  for (int j = 0; j < 8; ++j) {
    float s = rbf(bf2f(a[j]) + bf2f(b[j]));
    float o = rbf((j < 4 ? l0[j] : l1[j - 4]) + (j < 4 ? h0[j] : h1[j - 4]));
    float g = rbf(o * sigmoidf_(o));
    r[j] = f2bf(bf2f(xv[j]) + rbf(s * g));
  }
  *(u16x8*)(out + i) = r;
}

extern "C" void kernel_launch(void* const* d_in, const int* in_sizes, int n_in,
                              void* d_out, int out_size, void* d_ws, size_t ws_size,
                              hipStream_t stream) {
  const u16* x = (const u16*)d_in[0];
  const u16* sre = (const u16*)d_in[1];
  const u16* sim = (const u16*)d_in[2];
  const u16* wre = (const u16*)d_in[3];
  const u16* wim = (const u16*)d_in[4];
  const u16* wg = (const u16*)d_in[6];
  const u16* wu = (const u16*)d_in[7];
  const u16* wd = (const u16*)d_in[8];
  char* ws = (char*)d_ws;
  const size_t SD2 = (size_t)S_ * D_ * 2;
  u16* xn = (u16*)(ws);
  u16* a = (u16*)(ws + SD2);
  u16* nb = (u16*)(ws + 2 * SD2);
  u16* yre = (u16*)(ws + 3 * SD2);
  u16* ynim = (u16*)(ws + 4 * SD2);
  float* olo = (float*)(ws + 5 * SD2);
  float* ohi = (float*)(ws + 7 * SD2);
  u16* h = (u16*)(ws + 9 * SD2);
  (void)hipFuncSetAttribute((const void*)k1_glu, hipFuncAttributeMaxDynamicSharedMemorySize, K1_LDS);
  (void)hipFuncSetAttribute((const void*)k2_gemm, hipFuncAttributeMaxDynamicSharedMemorySize, K2_LDS);
  k0_ln<<<128 + 324, 256, 0, stream>>>(x, xn, (const char*)wg, (const char*)wu);
  k1_glu<<<256, 512, K1_LDS, stream>>>(xn, wg, wu, wre, wim, h, a, nb, (const char*)sre, (const char*)sim, (const char*)wd);
  k2_gemm<<<256, 512, K2_LDS, stream>>>(sre, sim, a, nb, h, wd, yre, ynim, olo, ohi);
  k3_combine<<<(S_ * D_ / 8) / 256, 256, 0, stream>>>(x, yre, ynim, olo, ohi, (u16*)d_out);
}
```
